```python
import torch

def _softmax(x, axis=-1):
    return torch.softmax(x, dim=axis)
def _gelu(x):
    return torch.nn.functional.gelu(x, approximate="tanh")
def _silu(x):
    return torch.nn.functional.silu(x)
def _sigmoid(x):
    return torch.sigmoid(x)
def _layernorm(x, axis=-1, eps=1e-5):
    m = x.mean(dim=axis, keepdim=True)
    v = x.var(dim=axis, keepdim=True, unbiased=False)
    return (x - m) / torch.sqrt(v + eps)
def _scatter_add(tgt, idx, src, axis=0):
    return tgt.clone().index_add_(axis, idx, src)
def _segment_sum(src, seg, n):
    out = torch.zeros((n,) + src.shape[1:], dtype=src.dtype)
    return out.index_add_(0, seg, src)
def _pad(x, pw):
    flat = tuple(v for pair in reversed(pw) for v in pair)
    return torch.nn.functional.pad(x, flat)

kernel_name = 'interleave_layers__ssm_conv_s4d__glu_gated_swiglu__prompt'

def setup_inputs():

    _w = (lambda a: (lambda t: t.to(torch.bfloat16) if t.is_floating_point() else t)(torch.as_tensor(a)))
    def _A_setup():
        Bsz, L, D, N = 1, 2048, 512, 128
        return {
            "u": _w((lambda *s: torch.randn(*s, dtype=torch.bfloat16) * 0.05)(Bsz, L, D)),
            "log_A_real": _w((lambda *s: torch.randn(*s, dtype=torch.bfloat16) * 3.2)(D, N) * 0.5),
            "A_imag": _w((lambda *s: torch.randn(*s, dtype=torch.bfloat16) * 0.05)(D, N) * 3.14),
            "C_real": _w((lambda *s: torch.randn(*s, dtype=torch.bfloat16) * 0.05)(D, N) / N ** 0.5),
            "C_imag": _w((lambda *s: torch.randn(*s, dtype=torch.bfloat16) * 0.05)(D, N) / N ** 0.5),
            "log_dt": _w((lambda *s: torch.randn(*s, dtype=torch.bfloat16) * 0.05)(D) * 0.5 - 4.0),
            "D_skip": _w((lambda *s: torch.randn(*s, dtype=torch.bfloat16) * 0.05)(D)),
        }
    _A_in = _A_setup()

    def _B_setup():
        B, S, D, F = 1, 2048, 512, 1376
        return {
            "x": _w((lambda *s: torch.randn(*s, dtype=torch.bfloat16) * 0.05)(B, S, D)),
            "w1": _w((lambda *s: torch.randn(*s, dtype=torch.bfloat16) * 0.4)(D, F) * (2.0 / D) ** 0.5),
            "w3": _w((lambda *s: torch.randn(*s, dtype=torch.bfloat16) * 0.4)(D, F) * (2.0 / D) ** 0.5),
            "w2": _w((lambda *s: torch.randn(*s, dtype=torch.bfloat16) * 0.4)(F, D) * (2.0 / F) ** 0.5),
        }
    _B_in = _B_setup()

    return {
        "A_u": _A_in["u"],
        "A_log_A_real": _A_in["log_A_real"],
        "A_A_imag": _A_in["A_imag"],
        "A_C_real": _A_in["C_real"],
        "A_C_imag": _A_in["C_imag"],
        "A_log_dt": _A_in["log_dt"],
        "A_D_skip": _A_in["D_skip"],
        "B_x": _B_in["x"],
        "B_w1": _B_in["w1"],
        "B_w3": _B_in["w3"],
        "B_w2": _B_in["w2"],
    }

def reference(A_u, A_log_A_real, A_A_imag, A_C_real, A_C_imag, A_log_dt, A_D_skip, B_x, B_w1, B_w3, B_w2):
    def _A_ref(u, log_A_real, A_imag, C_real, C_imag, log_dt, D_skip):
        L = u.shape[1]
        dt = torch.exp(log_dt)
        A = -torch.exp(log_A_real) + 1j * A_imag
        C = (C_real + 1j * C_imag) * dt[:, None]
        pos = (lambda x, dt: x.to(dt))(torch.arange(L), log_dt.dtype)
        P = torch.exp((A * dt[:, None])[:, :, None] * pos[None, None, :])
        K = 2.0 * torch.einsum("dn,dnl->dl", C, P).real
        L2 = 2 * L
        u_t = (lambda x, *d: x.permute(*d))(u, 0, 2, 1)
        Kf = (lambda x, *a, **k: torch.fft.rfft(x.float(), *a, **k))(K, L2)
        Uf = (lambda x, *a, **k: torch.fft.rfft(x.float(), *a, **k))(u_t, L2)
        y = (lambda x, *a, **k: torch.fft.irfft(x, *a, **k).to(torch.bfloat16))(Uf * Kf[None, :, :], L2)[:, :, :L]
        y = (lambda x, *d: x.permute(*d))(y, 0, 2, 1)
        return y + u * D_skip[None, None, :]

    def _B_ref(x, w1, w3, w2):
        gate = torch.einsum("bsd,df->bsf", x, w1)
        up = torch.einsum("bsd,df->bsf", x, w3)
        h = _silu(gate) * up
        out = torch.einsum("bsf,fd->bsd", h, w2)
        return out

    x = A_u
    x = x + _A_ref(_layernorm(x, axis=-1), A_log_A_real, A_A_imag, A_C_real, A_C_imag, A_log_dt, A_D_skip)
    x = x + _B_ref(_layernorm(x, axis=-1), B_w1, B_w3, B_w2)
    x = x + _A_ref(_layernorm(x, axis=-1), A_log_A_real, A_A_imag, A_C_real, A_C_imag, A_log_dt, A_D_skip)
    x = x + _B_ref(_layernorm(x, axis=-1), B_w1, B_w3, B_w2)
    return x
```

```cpp
#include <hip/hip_runtime.h>
#include <hip/hip_bf16.h>
#include <stdint.h>

typedef unsigned short u16;
typedef unsigned int u32;
typedef float f32x4 __attribute__((ext_vector_type(4)));
typedef float f32x2 __attribute__((ext_vector_type(2)));
typedef __bf16 bf16x8 __attribute__((ext_vector_type(8)));
typedef __bf16 bf16x2 __attribute__((ext_vector_type(2)));
typedef _Float16 f16x8 __attribute__((ext_vector_type(8)));
typedef _Float16 f16x2 __attribute__((ext_vector_type(2)));
typedef short s16x4 __attribute__((ext_vector_type(4)));
typedef u32 u32x4 __attribute__((ext_vector_type(4)));
typedef u32 u32x2 __attribute__((ext_vector_type(2)));
#define LDS_AS __attribute__((address_space(3)))
#define DEVI __device__ __forceinline__

constexpr int L_ = 2048, D_ = 512, N_ = 128, F_ = 1376;
constexpr int NFT = 15;
constexpr int NRT = NFT * 12;
constexpr int NKS2 = 44;
constexpr int KC_COPY = 4160;
constexpr int KC_CH = 4 * KC_COPY;

constexpr size_t OFF_XT0 = 0;
constexpr size_t OFF_X1T = OFF_XT0 + (size_t)L_ * D_ * 2;
constexpr size_t OFF_X2T = OFF_X1T + (size_t)L_ * D_ * 2;
constexpr size_t OFF_X3T = OFF_X2T + (size_t)L_ * D_ * 2;
constexpr size_t OFF_ST0 = OFF_X3T + (size_t)L_ * D_ * 2;
constexpr size_t OFF_ST2 = OFF_ST0 + (size_t)L_ * 8;
constexpr size_t OFF_KC = OFF_ST2 + (size_t)8 * L_ * 8;
constexpr size_t OFF_KINV = OFF_KC + (size_t)D_ * KC_CH;
constexpr size_t OFF_W13 = OFF_KINV + 2048;
constexpr size_t OFF_CS = OFF_W13 + (size_t)NRT * 16 * 1024;
constexpr size_t OFF_W2 = OFF_CS + (size_t)NRT * 16 * 4;
constexpr size_t OFF_H = OFF_W2 + (size_t)NKS2 * 32 * 1024;
constexpr size_t WS_END = OFF_H + (size_t)45 * 128 * 1024;

DEVI float bf2f(u16 v) { return __uint_as_float(((u32)v) << 16); }
DEVI float bflo(u32 v) { return __uint_as_float(v << 16); }
DEVI float bfhi(u32 v) { return __uint_as_float(v & 0xffff0000u); }
DEVI u32 pk_bf16(float lo, float hi) {
  f32x2 v = {lo, hi};
  bf16x2 b = __builtin_convertvector(v, bf16x2);
  return __builtin_bit_cast(u32, b);
}
DEVI float rbf(float x) { return (float)((__bf16)x); }
DEVI u32 pk_f16(float lo, float hi) {
  f16x2 h = {(_Float16)lo, (_Float16)hi};
  return __builtin_bit_cast(u32, h);
}
DEVI float wave_sum(float v) {
#pragma unroll
  for (int o = 32; o > 0; o >>= 1) v += __shfl_xor(v, o);
  return v;
}
DEVI float wave_max(float v) {
#pragma unroll
  for (int o = 32; o > 0; o >>= 1) v = fmaxf(v, __shfl_xor(v, o));
  return v;
}
DEVI float row16_sum(float v) {
#pragma unroll
  for (int o = 8; o > 0; o >>= 1) v += __shfl_xor(v, o);
  return v;
}
DEVI void ln_final(float mean, float var, float& m, float& s) {
  m = rbf(mean);
  float v = rbf(var);
  float ve = rbf(v + 1e-5f);
  s = rbf(sqrtf(ve));
}

struct cpx { float r, i; };
DEVI cpx cmul(cpx a, cpx b) { return {fmaf(a.r, b.r, -a.i * b.i), fmaf(a.r, b.i, a.i * b.r)}; }

constexpr int PB_KGEN = 0, PB_TR = 256, PB_ST = 512, PB_W13 = 640, PB_W2 = 760, PB_CS = 848,
              PB_END = 848 + 45;

constexpr int KG_ROWB = 544;

DEVI void kg_store(char* buf, int row, int lane, float re, float im) {
  float hr = rbf(re), hi = rbf(im);
  u32x2 v;
  v[0] = pk_bf16(hr, hi);
  v[1] = pk_bf16(re - hr, im - hi);
  *(u32x2*)(buf + row * KG_ROWB + lane * 8) = v;
}

DEVI void wave_fence() {
  __builtin_amdgcn_fence(__ATOMIC_RELEASE, "wavefront");
  __builtin_amdgcn_wave_barrier();
  __builtin_amdgcn_fence(__ATOMIC_ACQUIRE, "wavefront");
}

DEVI int kd_index(int l) {
  int p = l;
  if (l >= 256) {
    int s = 24 - __clz(l);
    int q = (l + (1 << (s - 1)) - 1 + ((l >> s) & 1)) >> s;
    p = q << s;
  }
  int j0 = (p >> 3) & 15, j1 = p >> 7, b = p & 7;
  int row;
  if (j1 < 2) row = 8 * j1 + b;
  else if (j1 < 4) row = 16 + 4 * (j1 - 2) + (b >> 1);
  else if (j1 < 8) row = 24 + 2 * (j1 - 4) + (b >> 2);
  else row = 32 + (j1 - 8);
  return row * 16 + j0;
}

__global__ __launch_bounds__(256) void prep_kernel(
    const u16* __restrict__ x0, const u16* __restrict__ logA, const u16* __restrict__ Aim,
    const u16* __restrict__ Cre, const u16* __restrict__ Cim, const u16* __restrict__ logdt,
    const u16* __restrict__ w1, const u16* __restrict__ w3, const u16* __restrict__ w2,
    char* __restrict__ ws) {
  __shared__ __attribute__((aligned(16))) char smem[4 * 16 * KG_ROWB + 4 * 48 * 16 * 4 + 64];
  const int tid = threadIdx.x, lane = tid & 63, wv = tid >> 6;
  const int b = blockIdx.x;
  if (b < PB_TR) {
    const int ch = wv >> 1;
    const int d = 2 * b + ch;
    const int n = 64 * (wv & 1) + lane;
    char* buf = smem + wv * 16 * KG_ROWB;
    float* tab = (float*)(smem + 4 * 16 * KG_ROWB);
    float* red = (float*)(smem + 4 * 16 * KG_ROWB + 4 * 48 * 16 * 4);
    const float dt = rbf(expf(bf2f(logdt[d])));
    const float ea = rbf(expf(bf2f(logA[d * N_ + n])));
    const float ar = -ea * dt;
    const float ai = bf2f(Aim[d * N_ + n]) * dt;
    cpx C = {2.0f * bf2f(Cre[d * N_ + n]) * dt, 2.0f * bf2f(Cim[d * N_ + n]) * dt};
    const float INV2PI_HI = 0.15915494f, INV2PI_LO = 6.4206382e-09f;
    const float L2E_HI = 1.44269502f, L2E_LO = 1.92596299e-08f;
    float t = ai * INV2PI_HI;
    float te = fmaf(ai, INV2PI_HI, -t) + ai * INV2PI_LO;
    float xl = ar * L2E_HI;
    float xe = fmaf(ar, L2E_HI, -xl) + ar * L2E_LO;
    cpx Bk[12];
#pragma unroll
    for (int k = 0; k < 12; ++k) {
      const float sc = (float)(1 << k);
      float tk = t * sc;
      float fr = tk - rintf(tk);
      float rk = fmaf(te, sc, fr);
      float mag = __builtin_amdgcn_exp2f(xl * sc) * fmaf(xe * sc, 0.693147181f, 1.0f);
      Bk[k].r = mag * __builtin_amdgcn_cosf(rk);
      Bk[k].i = mag * __builtin_amdgcn_sinf(rk);
    }
    const int r16 = lane & 15, g = lane >> 4;
    u32x4 bq[8];
    {
      cpx Q[16];
      Q[0] = {1.f, 0.f};
      Q[1] = Bk[3]; Q[2] = Bk[4]; Q[3] = cmul(Q[1], Bk[4]);
      Q[4] = Bk[5]; Q[5] = cmul(Q[1], Bk[5]); Q[6] = cmul(Q[2], Bk[5]); Q[7] = cmul(Q[3], Bk[5]);
      Q[8] = Bk[6];
#pragma unroll
      for (int j = 1; j < 8; ++j) Q[8 + j] = cmul(Q[j], Bk[6]);
#pragma unroll
      for (int j = 0; j < 16; ++j) kg_store(buf, j, lane, Q[j].r, Q[j].i);
      wave_fence();
#pragma unroll
      for (int s = 0; s < 8; ++s) bq[s] = *(const u32x4*)(buf + r16 * KG_ROWB + (8 * s + 2 * g) * 8);
      wave_fence();
    }
    cpx F[8];
    F[0] = {1.f, 0.f};
    F[1] = Bk[0]; F[2] = Bk[1]; F[3] = cmul(Bk[0], Bk[1]);
    F[4] = Bk[2]; F[5] = cmul(F[1], Bk[2]); F[6] = cmul(F[2], Bk[2]); F[7] = cmul(F[3], Bk[2]);
    cpx CG[17];
    CG[0] = C;
    CG[1] = cmul(C, Bk[7]); CG[2] = cmul(C, Bk[8]); CG[3] = cmul(CG[1], Bk[8]);
#pragma unroll
    for (int j = 0; j < 4; ++j) CG[4 + j] = cmul(CG[j], Bk[9]);
#pragma unroll
    for (int j = 0; j < 8; ++j) CG[8 + j] = cmul(CG[j], Bk[10]);
    CG[16] = cmul(C, Bk[11]);
#pragma unroll
    for (int tile = 0; tile < 3; ++tile) {
      if (tile == 0) {
#pragma unroll
        for (int j1 = 0; j1 < 2; ++j1)
#pragma unroll
          for (int bb = 0; bb < 8; ++bb) {
            cpx v = bb ? cmul(CG[j1], F[bb]) : CG[j1];
            kg_store(buf, 8 * j1 + bb, lane, v.r, -v.i);
          }
      } else if (tile == 1) {
#pragma unroll
        for (int j1 = 2; j1 < 4; ++j1)
#pragma unroll
          for (int bb = 0; bb < 4; ++bb) {
            cpx v = bb ? cmul(CG[j1], F[2 * bb]) : CG[j1];
            kg_store(buf, 4 * (j1 - 2) + bb, lane, v.r, -v.i);
          }
#pragma unroll
        for (int j1 = 4; j1 < 8; ++j1)
#pragma unroll
          for (int bb = 0; bb < 2; ++bb) {
            cpx v = bb ? cmul(CG[j1], F[4]) : CG[j1];
            kg_store(buf, 8 + 2 * (j1 - 4) + bb, lane, v.r, -v.i);
          }
      } else {
#pragma unroll
        for (int j1 = 8; j1 < 17; ++j1) kg_store(buf, j1 - 8, lane, CG[j1].r, -CG[j1].i);
      }
      wave_fence();
      f32x4 acc = {0.f, 0.f, 0.f, 0.f};
#pragma unroll
      for (int s = 0; s < 8; ++s) {
        u32x4 a = *(const u32x4*)(buf + r16 * KG_ROWB + (8 * s + 2 * g) * 8);
        u32x4 b1 = {bq[s][0], bq[s][0], bq[s][2], bq[s][2]};
        u32x4 b2 = {bq[s][1], bq[s][1], bq[s][3], bq[s][3]};
        acc = __builtin_amdgcn_mfma_f32_16x16x32_bf16(__builtin_bit_cast(bf16x8, a),
                                                      __builtin_bit_cast(bf16x8, b1), acc, 0, 0, 0);
        acc = __builtin_amdgcn_mfma_f32_16x16x32_bf16(__builtin_bit_cast(bf16x8, a),
                                                      __builtin_bit_cast(bf16x8, b2), acc, 0, 0, 0);
      }
      wave_fence();
#pragma unroll
      for (int r = 0; r < 4; ++r) tab[(wv * 48 + tile * 16 + 4 * g + r) * 16 + r16] = acc[r];
    }
    __syncthreads();
    const int t2 = tid & 127;
    float* t0 = tab + (2 * ch) * 768;
    float* t1 = tab + (2 * ch + 1) * 768;
    float mx = 0.f;
    for (int i = t2; i < 641; i += 128) {
      float v = t0[i] + t1[i];
      t0[i] = v;
      mx = fmaxf(mx, fabsf(v));
    }
    mx = wave_max(mx);
    if (lane == 0) red[wv] = mx;
    __syncthreads();
    mx = fmaxf(red[2 * ch], red[2 * ch + 1]);
    int e = 0;
    if (mx > 0.f) {
      e = 14 - (((int)((__float_as_uint(mx) >> 23) & 0xff)) - 127);
      e = e > 100 ? 100 : (e < -100 ? -100 : e);
    }
    const float scale = __uint_as_float((u32)(127 + e) << 23);
    if (t2 == 0) ((float*)(ws + OFF_KINV))[d] = __uint_as_float((u32)(127 - e) << 23);
    char* kc = ws + OFF_KC + (size_t)d * KC_CH;
    for (int q = t2; q < 258; q += 128) {
      float v[11];
#pragma unroll
      for (int i = 0; i < 11; ++i) {
        int l = 8 * q + i - 16;
        bool ok = (l >= 0) && (l < L_);
        int idx = kd_index(ok ? l : 0);
        v[i] = ok ? t0[idx] * scale : 0.f;
      }
#pragma unroll
      for (int s = 0; s < 4; ++s) {
        u32x4 o;
#pragma unroll
        for (int i = 0; i < 4; ++i) o[i] = pk_f16(v[s + 2 * i], v[s + 2 * i + 1]);
        *(u32x4*)(kc + s * KC_COPY + q * 16) = o;
      }
    }
  } else if (b < PB_ST) {
    const int tb = b - PB_TR;
    const int l0 = (tb >> 3) * 64, d0 = (tb & 7) * 64;
    u16* sm = (u16*)smem;
    constexpr int ST = 66;
#pragma unroll
    for (int it = 0; it < 2; ++it) {
      int idx = tid + it * 256;
      int r = idx >> 3, c = (idx & 7) * 8;
      u32x4 v = *(const u32x4*)(x0 + (size_t)(l0 + r) * D_ + d0 + c);
#pragma unroll
      for (int i = 0; i < 4; ++i) *(u32*)(sm + r * ST + c + 2 * i) = v[i];
    }
    __syncthreads();
    u16* xt = (u16*)(ws + OFF_XT0);
#pragma unroll
    for (int it = 0; it < 2; ++it) {
      int idx = tid + it * 256;
      int dd = idx >> 3, lc = (idx & 7) * 8;
      u32x4 o;
#pragma unroll
      for (int i = 0; i < 4; ++i)
        o[i] = (u32)sm[(lc + 2 * i) * ST + dd] | ((u32)sm[(lc + 2 * i + 1) * ST + dd] << 16);
      *(u32x4*)(xt + (size_t)(d0 + dd) * L_ + l0 + lc) = o;
    }
  } else if (b < PB_W13) {
    const int rb = (b - PB_ST) * 16 + wv * 4;
    u32x4 v[4];
#pragma unroll
    for (int i = 0; i < 4; ++i) v[i] = *(const u32x4*)(x0 + (size_t)(rb + i) * D_ + lane * 8);
#pragma unroll
    for (int i = 0; i < 4; ++i) {
      float f[8];
#pragma unroll
      for (int j = 0; j < 4; ++j) { f[2 * j] = bflo(v[i][j]); f[2 * j + 1] = bfhi(v[i][j]); }
      float s = 0.f;
#pragma unroll
      for (int j = 0; j < 8; ++j) s += f[j];
      float mean = wave_sum(s) * (1.0f / D_);
      float q = 0.f;
#pragma unroll
      for (int j = 0; j < 8; ++j) { float dlt = f[j] - mean; q = fmaf(dlt, dlt, q); }
      float var = wave_sum(q) * (1.0f / D_);
      float m, sd;
      ln_final(mean, var, m, sd);
      if (lane == 0) ((float2*)(ws + OFF_ST0))[rb + i] = make_float2(m, sd);
    }
  } else if (b < PB_W2) {
    const int bb = b - PB_W13;
    const int ft = bb >> 3, kp = bb & 7;
    u16* sm = (u16*)smem;
    constexpr int ST = 98;
#pragma unroll
    for (int it = 0; it < 6; ++it) {
      int idx = tid + it * 256;
      int mat = idx / 768, rem = idx % 768;
      int r = rem / 12, c = (rem % 12) * 8;
      int f = ft * 96 + c;
      u32x4 v = {0u, 0u, 0u, 0u};
      if (f < F_) v = *(const u32x4*)((mat ? w3 : w1) + (size_t)(kp * 64 + r) * F_ + f);
#pragma unroll
      for (int i = 0; i < 4; ++i) *(u32*)(sm + (mat * 64 + r) * ST + c + 2 * i) = v[i];
    }
    __syncthreads();
    char* wo = ws + OFF_W13;
#pragma unroll
    for (int it = 0; it < 6; ++it) {
      int idx = tid + it * 256;
      int fr = idx >> 6, ln = idx & 63;
      int rt = fr >> 1, kk = fr & 1;
      int row = ln & 15, g = ln >> 4;
      int mat = (row >> 1) & 1;
      int fl = 8 * rt + 2 * (row >> 2) + (row & 1);
      int dl = 32 * kk + 8 * g;
      u32x4 o;
#pragma unroll
      for (int i = 0; i < 4; ++i)
        o[i] = (u32)sm[(mat * 64 + dl + 2 * i) * ST + fl] |
               ((u32)sm[(mat * 64 + dl + 2 * i + 1) * ST + fl] << 16);
      *(u32x4*)(wo + ((size_t)(ft * 12 + rt) * 16 + kp * 2 + kk) * 1024 + ln * 16) = o;
    }
  } else if (b < PB_CS) {
    const int bb = b - PB_W2;
    const int ks = bb >> 1, hd = bb & 1;
    u16* sm = (u16*)smem;
    constexpr int ST = 258;
#pragma unroll
    for (int it = 0; it < 4; ++it) {
      int idx = tid + it * 256;
      int r = idx >> 5, c = (idx & 31) * 8;
      int f = ks * 32 + r;
      u32x4 v = {0u, 0u, 0u, 0u};
      if (f < F_) v = *(const u32x4*)(w2 + (size_t)f * D_ + hd * 256 + c);
#pragma unroll
      for (int i = 0; i < 4; ++i) *(u32*)(sm + r * ST + c + 2 * i) = v[i];
    }
    __syncthreads();
    char* wo = ws + OFF_W2;
#pragma unroll
    for (int it = 0; it < 4; ++it) {
      int idx = tid + it * 256;
      int dt = idx >> 6, ln = idx & 63;
      int dd = 16 * dt + (ln & 15), g = ln >> 4;
      u32x4 o;
#pragma unroll
      for (int rt = 0; rt < 4; ++rt)
        o[rt] = (u32)sm[(8 * rt + 2 * g) * ST + dd] | ((u32)sm[(8 * rt + 2 * g + 1) * ST + dd] << 16);
      *(u32x4*)(wo + ((size_t)ks * 32 + hd * 16 + dt) * 1024 + ln * 16) = o;
    }
  } else {
    const int bb = b - PB_CS;
    const int mat = (tid >> 4) & 1, fp = tid & 15, rg = tid >> 5;
    const int f = bb * 32 + 2 * fp;
    float s0 = 0.f, s1 = 0.f;
    if (f < F_) {
      const u16* w = (mat ? w3 : w1) + (size_t)(rg * 64) * F_ + f;
#pragma unroll 16
      for (int r = 0; r < 64; ++r) {
        u32 v = *(const u32*)(w + (size_t)r * F_);
        s0 += bflo(v);
        s1 += bfhi(v);
      }
    }
    float* sm = (float*)smem;
    sm[rg * 64 + (tid & 31) * 2] = s0;
    sm[rg * 64 + (tid & 31) * 2 + 1] = s1;
    __syncthreads();
    if (tid < 64) {
      float s = 0.f;
#pragma unroll
      for (int r = 0; r < 8; ++r) s += sm[r * 64 + tid];
      int m2 = tid >> 5, fo = tid & 31;
      int ff = bb * 32 + fo;
      int R = ff >> 3, fi = ff & 7;
      int row = 4 * (fi >> 1) + 2 * m2 + (fi & 1);
      ((float*)(ws + OFF_CS))[R * 16 + row] = s;
    }
  }
}

constexpr int CV_UR = 4608;
constexpr int CV_XS = 8192;
template <int NPART>
__global__ __launch_bounds__(256) void conv_kernel(const u16* __restrict__ xT,
                                                   const float* __restrict__ stats,
                                                   const char* __restrict__ KC,
                                                   const float* __restrict__ kinv,
                                                   const u16* __restrict__ Dskip,
                                                   u16* __restrict__ xoT) {
  __shared__ __attribute__((aligned(16))) char smem[2 * KC_CH + 2 * CV_UR + 2 * CV_XS + 4 * 4096];
  char* s_kc = smem;
  char* s_ur = smem + 2 * KC_CH;
  char* s_xs = s_ur + 2 * CV_UR;
  char* s_red = s_xs + 2 * CV_XS;
  const int tid = threadIdx.x, lane = tid & 63, wv = tid >> 6;
  const int d0 = 2 * blockIdx.x;
  u32x4 xv[2];
#pragma unroll
  for (int c = 0; c < 2; ++c) xv[c] = *(const u32x4*)(xT + (size_t)(d0 + c) * L_ + 8 * tid);
  float m[8], s[8];
  if (NPART == 1) {
    const f32x4* sp = (const f32x4*)(stats + 16 * tid);
#pragma unroll
    for (int i = 0; i < 4; ++i) {
      f32x4 v = sp[i];
      m[2 * i] = v[0]; s[2 * i] = v[1]; m[2 * i + 1] = v[2]; s[2 * i + 1] = v[3];
    }
  }
  f32x4 pv[NPART == 1 ? 1 : NPART][4];
  if (NPART > 1) {
#pragma unroll
    for (int p = 0; p < NPART; ++p) {
      const f32x4* sp = (const f32x4*)(stats + (size_t)p * L_ * 2 + 16 * tid);
#pragma unroll
      for (int i = 0; i < 4; ++i) pv[p][i] = sp[i];
    }
  }
  {
    const char* src = KC + (size_t)d0 * KC_CH;
    u32x4 kv[9];
#pragma unroll
    for (int i = 0; i < 8; ++i) kv[i] = *(const u32x4*)(src + (tid + 256 * i) * 16);
    if (tid < 32) kv[8] = *(const u32x4*)(src + (tid + 2048) * 16);
#pragma unroll
    for (int i = 0; i < 8; ++i) *(u32x4*)(s_kc + (tid + 256 * i) * 16) = kv[i];
    if (tid < 32) *(u32x4*)(s_kc + (tid + 2048) * 16) = kv[8];
  }
  if (NPART > 1) {
#pragma unroll
    for (int i = 0; i < 8; ++i) {
      float mean = 0.f;
#pragma unroll
      for (int p = 0; p < NPART; ++p) mean += pv[p][i >> 1][2 * (i & 1)];
      mean *= (1.0f / NPART);
      float M2 = 0.f, dv = 0.f;
#pragma unroll
      for (int p = 0; p < NPART; ++p) {
        M2 += pv[p][i >> 1][2 * (i & 1) + 1];
        float dl = pv[p][i >> 1][2 * (i & 1)] - mean;
        dv = fmaf(dl, dl, dv);
      }
      float var = (M2 + dv * (float)(D_ / NPART)) * (1.0f / D_);
      ln_final(mean, var, m[i], s[i]);
    }
  }
  float rs[8];
#pragma unroll
  for (int i = 0; i < 8; ++i) rs[i] = __builtin_amdgcn_rcpf(s[i]);
#pragma unroll
  for (int c = 0; c < 2; ++c) {
    const float dsk = bf2f(Dskip[d0 + c]);
    float u[8];
    u32x4 xs0, xs1;
#pragma unroll
    for (int i = 0; i < 8; ++i) {
      float x = (i & 1) ? bfhi(xv[c][i >> 1]) : bflo(xv[c][i >> 1]);
      float a = rbf(x - m[i]);
      float q = a * rs[i];
      float r = fmaf(-q, s[i], a);
      q = fmaf(r, rs[i], q);
      u[i] = rbf(q);
      u32 pk = pk_bf16(x, u[i] * dsk);
      if (i < 4) xs0[i] = pk; else xs1[i - 4] = pk;
    }
    u32x4 ur;
#pragma unroll
    for (int i = 0; i < 4; ++i) ur[i] = pk_f16(u[7 - 2 * i], u[6 - 2 * i]);
    *(u32x4*)(s_ur + c * CV_UR + (255 - tid) * 16) = ur;
    *(u32x4*)(s_xs + c * CV_XS + tid * 32) = xs0;
    *(u32x4*)(s_xs + c * CV_XS + tid * 32 + 16) = xs1;
    if (tid < 32) {
      u32x4 z = {0u, 0u, 0u, 0u};
      *(u32x4*)(s_ur + c * CV_UR + (256 + tid) * 16) = z;
    }
  }
  __syncthreads();
  const int ch = wv >> 1, hw = wv & 1;
  const int r16 = lane & 15, g = lane >> 4;
  const char* abase = s_kc + ch * KC_CH + ((1 + r16) & 3) * KC_COPY + 2 * (8 * g + ((1 + r16) & ~3));
  const char* bbase = s_ur + ch * CV_UR + 2 * (2032 - 16 * r16 + 8 * g) - 512 * 7;
  f32x4 acc[8];
#pragma unroll
  for (int j = 0; j < 8; ++j) acc[j] = (f32x4){0.f, 0.f, 0.f, 0.f};
#pragma unroll
  for (int wi = 0; wi < 4; ++wi) {
    const int w = 4 * hw + wi;
    f16x8 bf[8], af[8];
#pragma unroll
    for (int dl = 0; dl < 8; ++dl) bf[dl] = *(const f16x8*)(bbase + 512 * (7 - dl) + 64 * w);
#pragma unroll
    for (int cp = 0; cp < 8; ++cp) {
      u32x2 lo = *(const u32x2*)(abase + 64 * (8 * cp + w));
      u32x2 hi = *(const u32x2*)(abase + 64 * (8 * cp + w) + 8);
      u32x4 v = {lo[0], lo[1], hi[0], hi[1]};
      af[cp] = __builtin_bit_cast(f16x8, v);
    }
#pragma unroll
    for (int cp = 0; cp < 8; ++cp)
#pragma unroll
      for (int dl = 0; dl < 8 - cp; ++dl)
        acc[cp + dl] = __builtin_amdgcn_mfma_f32_16x16x32_f16(af[cp], bf[dl], acc[cp + dl], 0, 0, 0);
  }
  f32x4 keep[4];
  {
    f32x4* rp = (f32x4*)(s_red + wv * 4096) + lane;
#pragma unroll
    for (int j = 0; j < 4; ++j) {
      f32x4 snd;
#pragma unroll
      for (int r = 0; r < 4; ++r) {
        snd[r] = hw ? acc[j][r] : acc[4 + j][r];
        keep[j][r] = hw ? acc[4 + j][r] : acc[j][r];
      }
      rp[j * 64] = snd;
    }
  }
  __syncthreads();
  {
    const f32x4* rp = (const f32x4*)(s_red + (wv ^ 1) * 4096) + lane;
    const float ki = kinv[d0 + ch];
#pragma unroll
    for (int jj = 0; jj < 4; ++jj) {
      f32x4 o = rp[jj * 64];
      f32x4 a = keep[jj];
      const int t0 = 256 * (4 * hw + jj) + 16 * r16 + 4 * g;
      u32x4 xs = *(const u32x4*)(s_xs + ch * CV_XS + t0 * 4);
      float res[4];
#pragma unroll
      for (int r = 0; r < 4; ++r) {
        float y = rbf((a[r] + o[r]) * ki);
        float z = rbf(y + bfhi(xs[r]));
        res[r] = bflo(xs[r]) + z;
      }
      u32x2 ov = {pk_bf16(res[0], res[1]), pk_bf16(res[2], res[3])};
      *(u32x2*)(xoT + (size_t)(d0 + ch) * L_ + t0) = ov;
    }
  }
}

__global__ __launch_bounds__(256, 1) void gemm1_kernel(const u16* __restrict__ xT,
                                                       const char* __restrict__ W13,
                                                       const float* __restrict__ cs,
                                                       char* __restrict__ H) {
  __shared__ __attribute__((aligned(16))) char smem[512 * 256 + 128 * 8];
  float2* s_st = (float2*)(smem + 512 * 256);
  const int tid = threadIdx.x, lane = tid & 63, wv = tid >> 6;
  const int xcd = blockIdx.x & 7, idx = blockIdx.x >> 3;
  const int mt = 2 * xcd + (idx & 1), ft = idx >> 1;
  const int r16 = lane & 15, g = lane >> 4;
  u32x4 xr[4][8];
  const u16* xsrc = xT + (size_t)(32 * wv + g) * L_ + mt * 128 + r16 * 8;
  const char* wsrc = W13 + (size_t)(ft * 12 + 3 * wv) * 16 * 1024 + lane * 16;
  bf16x8 wf[16][3];
#pragma unroll
  for (int c = 0; c < 4; ++c) {
#pragma unroll
    for (int i = 0; i < 8; ++i) xr[c][i] = *(const u32x4*)(xsrc + (size_t)(128 * c + 4 * i) * L_);
    if (c < 2) {
#pragma unroll
      for (int k = 0; k < 4; ++k)
#pragma unroll
        for (int i = 0; i < 3; ++i)
          wf[4 * c + k][i] = *(const bf16x8*)(wsrc + (i * 16 + 4 * c + k) * 1024);
    }
  }
  const int q = (lane & 15) >> 2, p = lane & 3;
  const int hl = q | ((g & 1) << 2);
  int raddr[8];
#pragma unroll
  for (int j = 0; j < 8; ++j) raddr[j] = (8 * g + q) * 256 + 8 * p + ((((j + 2 * wv) & 7) ^ hl) * 32);
  f32x4 acc[3][8];
#pragma unroll
  for (int i = 0; i < 3; ++i)
#pragma unroll
    for (int lt = 0; lt < 8; ++lt) acc[i][lt] = (f32x4){0.f, 0.f, 0.f, 0.f};
  float s1[2] = {0.f, 0.f}, s2[2] = {0.f, 0.f};
  const bf16x2 ones = {(__bf16)1.0f, (__bf16)1.0f};

  auto write_chunk = [&](int c) {
#pragma unroll
    for (int i = 0; i < 8; ++i) {
      const int d = 128 * c + 32 * wv + 4 * i + g;
      const int h = g | (((i >> 1) & 1) << 2);
      *(u32x4*)(smem + d * 256 + (((r16 >> 1) ^ h) * 32) + (r16 & 1) * 16) = xr[c][i];
    }
  };
  write_chunk(0);
  __syncthreads();
#pragma unroll
  for (int c = 0; c < 4; ++c) {
#pragma unroll
    for (int k = 0; k < 4; ++k) {
      const int ks = 4 * c + k;
      if (k == 1 && c < 2) {
#pragma unroll
        for (int k2 = 0; k2 < 4; ++k2)
#pragma unroll
          for (int i = 0; i < 3; ++i)
            wf[4 * (c + 2) + k2][i] = *(const bf16x8*)(wsrc + (i * 16 + 4 * (c + 2) + k2) * 1024);
      }
      if (k == 2 && c < 3) write_chunk(c + 1);
      bf16x8 uf[8];
#pragma unroll
      for (int lt = 0; lt < 8; ++lt) {
        s16x4 lo = __builtin_amdgcn_ds_read_tr16_b64_v4i16(
            (LDS_AS s16x4*)(smem + raddr[lt] + (32 * ks) * 256));
        s16x4 hi = __builtin_amdgcn_ds_read_tr16_b64_v4i16(
            (LDS_AS s16x4*)(smem + raddr[lt] + (32 * ks + 4) * 256));
        u32x2 a = __builtin_bit_cast(u32x2, lo), b2 = __builtin_bit_cast(u32x2, hi);
        u32x4 v = {a[0], a[1], b2[0], b2[1]};
        uf[lt] = __builtin_bit_cast(bf16x8, v);
      }
#pragma unroll
      for (int e = 0; e < 2; ++e) {
        bf16x8 sv = uf[e];
        u32x4 sw = __builtin_bit_cast(u32x4, sv);
#pragma unroll
        for (int j = 0; j < 4; ++j) {
          const u32 swj = sw[j];
          bf16x2 pr = __builtin_bit_cast(bf16x2, swj);
          s1[e] = __builtin_amdgcn_fdot2_f32_bf16(pr, ones, s1[e], false);
          s2[e] = __builtin_amdgcn_fdot2_f32_bf16(pr, pr, s2[e], false);
        }
      }
#pragma unroll
      for (int lt = 0; lt < 8; ++lt)
#pragma unroll
        for (int i = 0; i < 3; ++i)
          acc[i][lt] = __builtin_amdgcn_mfma_f32_16x16x32_bf16(wf[ks][i], uf[lt], acc[i][lt], 0, 0, 0);
    }
    if (c < 3) __syncthreads();
  }
#pragma unroll
  for (int e = 0; e < 2; ++e) {
    float a = s1[e], b2 = s2[e];
    a += __shfl_xor(a, 16); a += __shfl_xor(a, 32);
    b2 += __shfl_xor(b2, 16); b2 += __shfl_xor(b2, 32);
    float mean = a * (1.0f / D_);
    float var = fmaxf(b2 * (1.0f / D_) - mean * mean, 0.f);
    float m, sd;
    ln_final(mean, var, m, sd);
    if (g == 0) s_st[(2 * wv + e) * 16 + r16] = make_float2(m, 1.0f / sd);
  }
  __syncthreads();
#pragma unroll
  for (int i = 0; i < 3; ++i) {
    const int R = ft * 12 + 3 * wv + i;
    const f32x4 c4 = *(const f32x4*)(cs + R * 16 + 4 * g);
    char* hp = H + ((size_t)(R >> 2) * 128 + mt * 8) * 1024 + lane * 16 + (R & 3) * 4;
#pragma unroll
    for (int j = 0; j < 8; ++j) {
      const int lt = (j + 2 * wv) & 7;
      float2 st = s_st[lt * 16 + r16];
      float hv[2];
#pragma unroll
      for (int e = 0; e < 2; ++e) {
        float gt = rbf((acc[i][j][e] - st.x * c4[e]) * st.y);
        float up = rbf((acc[i][j][2 + e] - st.x * c4[2 + e]) * st.y);
        float sg = rbf(gt * __builtin_amdgcn_rcpf(1.0f + __expf(-gt)));
        hv[e] = sg * up;
      }
      *(u32*)(hp + lt * 1024) = pk_bf16(hv[0], hv[1]);
    }
  }
}

template <int FINAL>
__global__ __launch_bounds__(256, 1) void gemm2_kernel(const char* __restrict__ H,
                                                       const char* __restrict__ W2,
                                                       const u16* __restrict__ xT,
                                                       u16* __restrict__ xoT,
                                                       float* __restrict__ stats,
                                                       u16* __restrict__ out) {
  __shared__ __attribute__((aligned(16))) char smem[4 * 12 * 1024 + 4 * 16 * 144];
  const int tid = threadIdx.x, lane = tid & 63, wv = tid >> 6;
  const int xcd = blockIdx.x & 7, idx = blockIdx.x >> 3;
  const int mt = 4 * xcd + (idx & 3), nt = idx >> 2;
  const int r16 = lane & 15, g = lane >> 4;
  const char* hsrc = H + ((size_t)wv * 128 + 4 * mt) * 1024 + lane * 16;
  const char* wsrc = W2 + ((size_t)wv * 32 + 4 * nt) * 1024 + lane * 16;
  constexpr int NS = 11, PF = 7;
  bf16x8 hf[NS][4], wf[NS][4];
  auto load_step = [&](int s) {
#pragma unroll
    for (int a = 0; a < 4; ++a) hf[s][a] = *(const bf16x8*)(hsrc + ((size_t)s * 4 * 128 + a) * 1024);
#pragma unroll
    for (int a = 0; a < 4; ++a) wf[s][a] = *(const bf16x8*)(wsrc + ((size_t)s * 4 * 32 + a) * 1024);
  };
#pragma unroll
  for (int s = 0; s < PF; ++s) load_step(s);
  const int lrow = 64 * mt + 16 * wv + 4 * g;
  u32x2 xres[4];
#pragma unroll
  for (int b2 = 0; b2 < 4; ++b2)
    xres[b2] = *(const u32x2*)(xT + (size_t)(64 * nt + 16 * b2 + r16) * L_ + lrow);
  f32x4 acc[4][4];
#pragma unroll
  for (int a = 0; a < 4; ++a)
#pragma unroll
    for (int b2 = 0; b2 < 4; ++b2) acc[a][b2] = (f32x4){0.f, 0.f, 0.f, 0.f};
#pragma unroll
  for (int s = 0; s < NS; ++s) {
    if (s + PF < NS) load_step(s + PF);
#pragma unroll
    for (int a = 0; a < 4; ++a)
#pragma unroll
      for (int b2 = 0; b2 < 4; ++b2)
        acc[a][b2] = __builtin_amdgcn_mfma_f32_16x16x32_bf16(hf[s][a], wf[s][b2], acc[a][b2], 0, 0, 0);
  }
  f32x4 own[4];
#pragma unroll
  for (int a = 0; a < 4; ++a) {
    if (a == wv) {
#pragma unroll
      for (int b2 = 0; b2 < 4; ++b2) own[b2] = acc[a][b2];
    } else {
      const int src = (wv - a - 1) & 3;
      f32x4* dst = (f32x4*)(smem + (a * 12 + src * 4) * 1024) + lane;
#pragma unroll
      for (int b2 = 0; b2 < 4; ++b2) dst[b2 * 64] = acc[a][b2];
    }
  }
  __syncthreads();
  {
    const f32x4* sp = (const f32x4*)(smem + wv * 12 * 1024) + lane;
#pragma unroll
    for (int src = 0; src < 3; ++src)
#pragma unroll
      for (int b2 = 0; b2 < 4; ++b2) own[b2] += sp[(src * 4 + b2) * 64];
  }
  float xo[4][4];
#pragma unroll
  for (int b2 = 0; b2 < 4; ++b2)
#pragma unroll
    for (int r = 0; r < 4; ++r) {
      float xr = (r & 1) ? bfhi(xres[b2][r >> 1]) : bflo(xres[b2][r >> 1]);
      xo[b2][r] = rbf(xr + rbf(own[b2][r]));
    }
  if (!FINAL) {
#pragma unroll
    for (int b2 = 0; b2 < 4; ++b2) {
      u32x2 ov = {pk_bf16(xo[b2][0], xo[b2][1]), pk_bf16(xo[b2][2], xo[b2][3])};
      *(u32x2*)(xoT + (size_t)(64 * nt + 16 * b2 + r16) * L_ + lrow) = ov;
    }
    f32x4 o0, o1;
#pragma unroll
    for (int r = 0; r < 4; ++r) {
      float sm = row16_sum(xo[0][r] + xo[1][r] + xo[2][r] + xo[3][r]) * (1.0f / 64.0f);
      float q = 0.f;
#pragma unroll
      for (int b2 = 0; b2 < 4; ++b2) { float dl = xo[b2][r] - sm; q = fmaf(dl, dl, q); }
      q = row16_sum(q);
      if (r < 2) { o0[2 * r] = sm; o0[2 * r + 1] = q; } else { o1[2 * (r - 2)] = sm; o1[2 * (r - 2) + 1] = q; }
    }
    if (r16 == 0) {
      f32x4* sp = (f32x4*)(stats + ((size_t)nt * L_ + lrow) * 2);
      sp[0] = o0;
      sp[1] = o1;
    }
  } else {
    char* tp = smem + 4 * 12 * 1024 + wv * 16 * 144;
#pragma unroll
    for (int b2 = 0; b2 < 4; ++b2)
#pragma unroll
      for (int r = 0; r < 4; ++r)
        *(u16*)(tp + (4 * g + r) * 144 + (16 * b2 + r16) * 2) = (u16)(__float_as_uint(xo[b2][r]) >> 16);
    wave_fence();
#pragma unroll
    for (int it = 0; it < 2; ++it) {
      const int row = (lane >> 3) + 8 * it, cc = lane & 7;
      u32x4 v = *(const u32x4*)(tp + row * 144 + cc * 16);
      *(u32x4*)(out + (size_t)(64 * mt + 16 * wv + row) * D_ + 64 * nt + cc * 8) = v;
    }
  }
}

extern "C" void kernel_launch(void* const* d_in, const int* in_sizes, int n_in, void* d_out,
                              int out_size, void* d_ws, size_t ws_size, hipStream_t stream) {
  const u16* x0 = (const u16*)d_in[0];
  const u16* logA = (const u16*)d_in[1];
  const u16* Aim = (const u16*)d_in[2];
  const u16* Cre = (const u16*)d_in[3];
  const u16* Cim = (const u16*)d_in[4];
  const u16* logdt = (const u16*)d_in[5];
  const u16* Dsk = (const u16*)d_in[6];
  const u16* w1 = (const u16*)d_in[8];
  const u16* w3 = (const u16*)d_in[9];
  const u16* w2 = (const u16*)d_in[10];
  char* ws = (char*)d_ws;
  u16* out = (u16*)d_out;
  prep_kernel<<<PB_END, 256, 0, stream>>>(x0, logA, Aim, Cre, Cim, logdt, w1, w3, w2, ws);
  conv_kernel<1><<<256, 256, 0, stream>>>((const u16*)(ws + OFF_XT0), (const float*)(ws + OFF_ST0),
                                          ws + OFF_KC, (const float*)(ws + OFF_KINV), Dsk,
                                          (u16*)(ws + OFF_X1T));
  gemm1_kernel<<<240, 256, 0, stream>>>((const u16*)(ws + OFF_X1T), ws + OFF_W13,
                                        (const float*)(ws + OFF_CS), ws + OFF_H);
  gemm2_kernel<0><<<256, 256, 0, stream>>>(ws + OFF_H, ws + OFF_W2, (const u16*)(ws + OFF_X1T),
                                           (u16*)(ws + OFF_X2T), (float*)(ws + OFF_ST2), out);
  conv_kernel<8><<<256, 256, 0, stream>>>((const u16*)(ws + OFF_X2T), (const float*)(ws + OFF_ST2),
                                          ws + OFF_KC, (const float*)(ws + OFF_KINV), Dsk,
                                          (u16*)(ws + OFF_X3T));
  gemm1_kernel<<<240, 256, 0, stream>>>((const u16*)(ws + OFF_X3T), ws + OFF_W13,
                                        (const float*)(ws + OFF_CS), ws + OFF_H);
  gemm2_kernel<1><<<256, 256, 0, stream>>>(ws + OFF_H, ws + OFF_W2, (const u16*)(ws + OFF_X3T),
                                           (u16*)(ws + OFF_X2T), (float*)(ws + OFF_ST2), out);
}
```

```cpp
#include <hip/hip_runtime.h>
#include <hip/hip_bf16.h>
#include <stdint.h>
#include <type_traits>

typedef unsigned short u16;
typedef unsigned int u32;
typedef float f32x4 __attribute__((ext_vector_type(4)));
typedef float f32x2 __attribute__((ext_vector_type(2)));
typedef __bf16 bf16x8 __attribute__((ext_vector_type(8)));
typedef __bf16 bf16x2 __attribute__((ext_vector_type(2)));
typedef _Float16 f16x8 __attribute__((ext_vector_type(8)));
typedef _Float16 f16x2 __attribute__((ext_vector_type(2)));
typedef short s16x4 __attribute__((ext_vector_type(4)));
typedef u32 u32x4 __attribute__((ext_vector_type(4)));
typedef u32 u32x2 __attribute__((ext_vector_type(2)));
#define LDS_AS __attribute__((address_space(3)))
#define DEVI __device__ __forceinline__
#define SB0() __builtin_amdgcn_sched_barrier(0)

#define DIAG_ARG
#define DIAG_PASS(k)
#define STAMP_BEGIN()
#define STAMP(k)
#define STAMP_END()

template <int I, int N, class F>
DEVI void static_for(F&& f) {
  if constexpr (I < N) {
    f(std::integral_constant<int, I>{});
    static_for<I + 1, N>(f);
  }
}

constexpr int L_ = 2048, D_ = 512, N_ = 128, F_ = 1376;
constexpr int NFT = 15;
constexpr int NRT = NFT * 12;
constexpr int NKS2 = 44;
constexpr int KC_ROW = 4160;

constexpr size_t OFF_XT0 = 0;
constexpr size_t OFF_X1T = OFF_XT0 + (size_t)L_ * D_ * 2;
constexpr size_t OFF_X2T = OFF_X1T + (size_t)L_ * D_ * 2;
constexpr size_t OFF_X3T = OFF_X2T + (size_t)L_ * D_ * 2;
constexpr size_t OFF_ST0 = OFF_X3T + (size_t)L_ * D_ * 2;
constexpr size_t OFF_ST2 = OFF_ST0 + (size_t)L_ * 8;
constexpr size_t OFF_KC = OFF_ST2 + (size_t)8 * L_ * 8;
constexpr size_t OFF_KINV = OFF_KC + (size_t)D_ * KC_ROW;
constexpr size_t OFF_W13 = OFF_KINV + 2048;
constexpr size_t OFF_CS = OFF_W13 + (size_t)NRT * 16 * 1024;
constexpr size_t OFF_W2 = OFF_CS + (size_t)NRT * 16 * 4;
constexpr size_t OFF_H = OFF_W2 + (size_t)NKS2 * 32 * 1024;
constexpr size_t WS_END = OFF_H + (size_t)45 * 128 * 1024;

DEVI float bf2f(u16 v) { return __uint_as_float(((u32)v) << 16); }
DEVI float bflo(u32 v) { return __uint_as_float(v << 16); }
DEVI float bfhi(u32 v) { return __uint_as_float(v & 0xffff0000u); }
DEVI u32 pk_bf16(float lo, float hi) {
  f32x2 v = {lo, hi};
  bf16x2 b = __builtin_convertvector(v, bf16x2);
  return __builtin_bit_cast(u32, b);
}
DEVI float rbf(float x) { return (float)((__bf16)x); }
DEVI u32 pk_f16(float lo, float hi) {
  f16x2 h = {(_Float16)lo, (_Float16)hi};
  return __builtin_bit_cast(u32, h);
}
DEVI float wave_sum(float v) {
#pragma unroll
  for (int o = 32; o > 0; o >>= 1) v += __shfl_xor(v, o);
  return v;
}
DEVI float row16_sum(float v) {
#pragma unroll
  for (int o = 8; o > 0; o >>= 1) v += __shfl_xor(v, o);
  return v;
}
DEVI void ln_final(float mean, float var, float& m, float& s) {
  m = rbf(mean);
  float v = rbf(var);
  float ve = rbf(v + 1e-5f);
  s = rbf(sqrtf(ve));
}
DEVI void st16_wt(void* p, u32x4 v) {
  asm volatile("global_store_dwordx4 %0, %1, off sc1\n\ts_nop 1" ::"v"(p), "v"(v) : "memory");
}
DEVI void raw_barrier() {
  asm volatile("s_waitcnt lgkmcnt(0)" ::: "memory");
  __builtin_amdgcn_s_barrier();
  asm volatile("" ::: "memory");
}
template <int LOFF>
DEVI void glds16(u32 voff, u32 ldsbase, const char* sbase) {
  asm volatile("s_add_u32 m0, %1, %2\n\ts_nop 0\n\tglobal_load_lds_dwordx4 %0, %3"
               ::"v"(voff), "s"(ldsbase), "i"(LOFF), "s"(sbase) : "memory", "scc");
}
template <int OFF>
DEVI void gld16(bf16x8& dst, u32 voff, const char* sbase) {
  asm volatile("global_load_dwordx4 %0, %1, %2 offset:%3" : "=v"(dst) : "v"(voff), "s"(sbase), "i"(OFF) : "memory");
}
template <int N>
DEVI void wait_vm() { asm volatile("s_waitcnt vmcnt(%0)" ::"i"(N) : "memory"); }
template <int N>
DEVI void wait_vm3(bf16x8& a, bf16x8& b, bf16x8& c) {
  asm volatile("s_waitcnt vmcnt(%3)" : "+v"(a), "+v"(b), "+v"(c) : "i"(N) : "memory");
}
DEVI int chan_of_block(int b) { return 256 * (b & 1) + 64 * ((b >> 1) & 3) + 2 * (b >> 3); }

struct cpx { float r, i; };
DEVI cpx cmul(cpx a, cpx b) { return {fmaf(a.r, b.r, -a.i * b.i), fmaf(a.r, b.i, a.i * b.r)}; }

constexpr int PB_TR = 256, PB_ST = 512, PB_W13 = 640, PB_W2 = 760, PB_CS = 848, PB_END = 848 + 45;

constexpr int KG_ROWB = 544;

DEVI void kg_store(char* buf, int row, int lane, float re, float im) {
  u32x2 v;
  v[0] = pk_bf16(re, im);
  v[1] = pk_bf16(re - bflo(v[0]), im - bfhi(v[0]));
  *(u32x2*)(buf + row * KG_ROWB + lane * 8) = v;
}

DEVI void wave_fence() {
  __builtin_amdgcn_fence(__ATOMIC_RELEASE, "wavefront");
  __builtin_amdgcn_wave_barrier();
  __builtin_amdgcn_fence(__ATOMIC_ACQUIRE, "wavefront");
}

__global__ __launch_bounds__(256) void prep_kernel(
    const u16* __restrict__ x0, const u16* __restrict__ logA, const u16* __restrict__ Aim,
    const u16* __restrict__ Cre, const u16* __restrict__ Cim, const u16* __restrict__ logdt,
    const u16* __restrict__ w1, const u16* __restrict__ w3, const u16* __restrict__ w2,
    char* __restrict__ ws DIAG_ARG) {
  STAMP_BEGIN();
  __shared__ __attribute__((aligned(16))) char smem[4 * 16 * KG_ROWB + 64];
  const int tid = threadIdx.x, lane = tid & 63, wv = tid >> 6;
  const int b = blockIdx.x;
  if (b < PB_TR) {
    const int ch = wv >> 1;
    const int dbase = chan_of_block(b);
    const int d = dbase + ch;
    const int n = 64 * (wv & 1) + lane;
    char* buf = smem + wv * 16 * KG_ROWB;
    float* red = (float*)(smem + 4 * 16 * KG_ROWB);
    const float dt = rbf(expf(bf2f(logdt[d])));
    const float ea = rbf(expf(bf2f(logA[d * N_ + n])));
    const float ar = -ea * dt;
    const float ai = bf2f(Aim[d * N_ + n]) * dt;
    cpx C = {2.0f * bf2f(Cre[d * N_ + n]) * dt, 2.0f * bf2f(Cim[d * N_ + n]) * dt};
    {
      float bs = wave_sum(fabsf(C.r) + fabsf(C.i));
      if (lane == 0) red[wv] = bs;
    }
    const float INV2PI_HI = 0.15915494f, INV2PI_LO = 6.4206382e-09f;
    const float L2E_HI = 1.44269502f, L2E_LO = 1.92596299e-08f;
    float t = ai * INV2PI_HI;
    float te = fmaf(ai, INV2PI_HI, -t) + ai * INV2PI_LO;
    float xl = ar * L2E_HI;
    float xe = fmaf(ar, L2E_HI, -xl) + ar * L2E_LO;
    cpx Bk[12];
#pragma unroll
    for (int k = 0; k < 12; ++k) {
      const float sc = (float)(1 << k);
      float tk = t * sc;
      float fr = tk - rintf(tk);
      float rk = fmaf(te, sc, fr);
      float mag = __builtin_amdgcn_exp2f(xl * sc) * fmaf(xe * sc, 0.693147181f, 1.0f);
      Bk[k].r = mag * __builtin_amdgcn_cosf(rk);
      Bk[k].i = mag * __builtin_amdgcn_sinf(rk);
    }
    STAMP(2);
    const int r16 = lane & 15, g = lane >> 4;
    u32x4 bq[8], bs[8];
    {
      cpx E[4];
#pragma unroll
      for (int k = 0; k < 4; ++k) E[k] = {Bk[3 + k].r, -Bk[3 + k].i};
      cpx Q[16];
      Q[0] = {1.f, 0.f};
      Q[1] = E[0]; Q[2] = E[1]; Q[3] = cmul(Q[1], E[1]);
      Q[4] = E[2]; Q[5] = cmul(Q[1], E[2]); Q[6] = cmul(Q[2], E[2]); Q[7] = cmul(Q[3], E[2]);
      Q[8] = E[3];
#pragma unroll
      for (int j = 1; j < 8; ++j) Q[8 + j] = cmul(Q[j], E[3]);
#pragma unroll
      for (int j = 0; j < 16; ++j) kg_store(buf, j, lane, Q[j].r, Q[j].i);
      wave_fence();
#pragma unroll
      for (int s = 0; s < 8; ++s) {
        bq[s] = *(const u32x4*)(buf + r16 * KG_ROWB + (8 * s + 2 * g) * 8);
        bs[s] = (u32x4){bq[s][1], bq[s][0], bq[s][3], bq[s][2]};
      }
      wave_fence();
    }
    cpx F[8];
    F[0] = {1.f, 0.f};
    F[1] = Bk[0]; F[2] = Bk[1]; F[3] = cmul(Bk[0], Bk[1]);
    F[4] = Bk[2]; F[5] = cmul(F[1], Bk[2]); F[6] = cmul(F[2], Bk[2]); F[7] = cmul(F[3], Bk[2]);
    cpx CG[17];
    CG[0] = C;
    CG[1] = cmul(C, Bk[7]); CG[2] = cmul(C, Bk[8]); CG[3] = cmul(CG[1], Bk[8]);
#pragma unroll
    for (int j = 0; j < 4; ++j) CG[4 + j] = cmul(CG[j], Bk[9]);
#pragma unroll
    for (int j = 0; j < 8; ++j) CG[8 + j] = cmul(CG[j], Bk[10]);
    CG[16] = cmul(C, Bk[11]);
    f32x4 tacc[3];
#pragma unroll
    for (int tile = 0; tile < 3; ++tile) {
      if (tile == 0) {
#pragma unroll
        for (int j1 = 0; j1 < 2; ++j1)
#pragma unroll
          for (int bb = 0; bb < 8; ++bb) {
            cpx v = bb ? cmul(CG[j1], F[bb]) : CG[j1];
            kg_store(buf, 8 * j1 + bb, lane, v.r, v.i);
          }
      } else if (tile == 1) {
#pragma unroll
        for (int j1 = 2; j1 < 4; ++j1)
#pragma unroll
          for (int bb = 0; bb < 4; ++bb) {
            cpx v = bb ? cmul(CG[j1], F[2 * bb]) : CG[j1];
            kg_store(buf, 4 * (j1 - 2) + bb, lane, v.r, v.i);
          }
#pragma unroll
        for (int j1 = 4; j1 < 8; ++j1)
#pragma unroll
          for (int bb = 0; bb < 2; ++bb) {
            cpx v = bb ? cmul(CG[j1], F[4]) : CG[j1];
            kg_store(buf, 8 + 2 * (j1 - 4) + bb, lane, v.r, v.i);
          }
      } else {
#pragma unroll
        for (int j1 = 8; j1 < 17; ++j1) kg_store(buf, j1 - 8, lane, CG[j1].r, CG[j1].i);
      }
      wave_fence();
      f32x4 acc0 = {0.f, 0.f, 0.f, 0.f}, acc1 = {0.f, 0.f, 0.f, 0.f};
#pragma unroll
      for (int s = 0; s < 8; ++s) {
        u32x4 a = *(const u32x4*)(buf + r16 * KG_ROWB + (8 * s + 2 * g) * 8);
        acc0 = __builtin_amdgcn_mfma_f32_16x16x32_bf16(__builtin_bit_cast(bf16x8, a),
                                                       __builtin_bit_cast(bf16x8, bq[s]), acc0, 0, 0, 0);
        acc1 = __builtin_amdgcn_mfma_f32_16x16x32_bf16(__builtin_bit_cast(bf16x8, a),
                                                       __builtin_bit_cast(bf16x8, bs[s]), acc1, 0, 0, 0);
      }
      tacc[tile] = acc0 + acc1;
      wave_fence();
    }
    {
      float* tab = (float*)buf;
#pragma unroll
      for (int tile = 0; tile < 3; ++tile)
#pragma unroll
        for (int r = 0; r < 4; ++r) tab[(tile * 16 + 4 * g + r) * 16 + r16] = tacc[tile][r];
    }
    STAMP(3);
    __syncthreads();
    float scale[2];
#pragma unroll
    for (int c = 0; c < 2; ++c) {
      float mx = red[2 * c] + red[2 * c + 1];
      int e = 0;
      if (mx > 0.f) {
        e = 14 - (((int)((__float_as_uint(mx) >> 23) & 0xff)) - 127);
        e = e > 100 ? 100 : (e < -100 ? -100 : e);
      }
      scale[c] = __uint_as_float((u32)(127 + e) << 23);
      if (tid == c) ((float*)(ws + OFF_KINV))[dbase + c] = __uint_as_float((u32)(127 - e) << 23);
    }
    auto tabv = [&](int c, int idx) {
      const float* t0 = (const float*)(smem + (2 * c) * 16 * KG_ROWB);
      const float* t1 = (const float*)(smem + (2 * c + 1) * 16 * KG_ROWB);
      return (t0[idx] + t1[idx]) * (c ? scale[1] : scale[0]);
    };
    auto first_idx = [&](int q) {
      int j1 = q >> 4, j0 = q & 15;
      int row = j1 < 2 ? 8 * j1 : (j1 < 4 ? 16 + 4 * (j1 - 2) : (j1 < 8 ? 24 + 2 * (j1 - 4) : 32 + (j1 - 8)));
      return row * 16 + j0;
    };
    auto kout = [&](int c, int q, u32x4 o) {
      *(u32x4*)(ws + OFF_KC + (size_t)(dbase + c) * KC_ROW + 32 + q * 16) = o;
    };
    if (wv == 0) {
      const int c = lane >> 5, q = lane & 31;
      const int i0 = first_idx(q);
      float v[8];
#pragma unroll
      for (int i = 0; i < 8; ++i) v[i] = tabv(c, i0 + 16 * i);
      kout(c, q, (u32x4){pk_f16(v[0], v[1]), pk_f16(v[2], v[3]), pk_f16(v[4], v[5]), pk_f16(v[6], v[7])});
    } else if (wv == 1) {
      const int c = lane >> 5, q = 32 + (lane & 31);
      const int i0 = first_idx(q);
      float v0 = tabv(c, i0), v2 = tabv(c, i0 + 16), v4 = tabv(c, i0 + 32), v6 = tabv(c, i0 + 48);
      float v8 = tabv(c, first_idx(q + 1));
      kout(c, q, (u32x4){pk_f16(v0, v0), pk_f16(v2, v4), pk_f16(v4, v4), pk_f16(v6, v8)});
    } else {
      const int c = wv - 2, q = 64 + lane;
      const int i0 = first_idx(q);
      float v0 = tabv(c, i0), v4 = tabv(c, i0 + 16), v8 = tabv(c, first_idx(q + 1));
      kout(c, q, (u32x4){pk_f16(v0, v0), pk_f16(v0, v4), pk_f16(v4, v4), pk_f16(v8, v8)});
    }
    {
      const int c = wv >> 1, q = 128 + 64 * (wv & 1) + lane;
      float v0 = tabv(c, first_idx(q)), v8 = tabv(c, first_idx(q + 1));
      float vm = (q & 1) ? v8 : v0;
      kout(c, q, (u32x4){pk_f16(v0, v0), pk_f16(v0, v0), pk_f16(vm, v8), pk_f16(v8, v8)});
    }
    if (tid < 8) {
      const int c = tid >> 2, p = tid & 3;
      kout(c, p < 2 ? p - 2 : 254 + p, (u32x4){0u, 0u, 0u, 0u});
    }
  } else if (b < PB_ST) {
    const int tb = b - PB_TR;
    const int l0 = (tb >> 3) * 64, d0 = (tb & 7) * 64;
    u16* sm = (u16*)smem;
    constexpr int ST = 66;
#pragma unroll
    for (int it = 0; it < 2; ++it) {
      int idx = tid + it * 256;
      int r = idx >> 3, c = (idx & 7) * 8;
      u32x4 v = *(const u32x4*)(x0 + (size_t)(l0 + r) * D_ + d0 + c);
#pragma unroll
      for (int i = 0; i < 4; ++i) *(u32*)(sm + r * ST + c + 2 * i) = v[i];
    }
    __syncthreads();
    u16* xt = (u16*)(ws + OFF_XT0);
#pragma unroll
    for (int it = 0; it < 2; ++it) {
      int idx = tid + it * 256;
      int dd = idx >> 3, lc = (idx & 7) * 8;
      u32x4 o;
#pragma unroll
      for (int i = 0; i < 4; ++i)
        o[i] = (u32)sm[(lc + 2 * i) * ST + dd] | ((u32)sm[(lc + 2 * i + 1) * ST + dd] << 16);
      st16_wt(xt + (size_t)(d0 + dd) * L_ + l0 + lc, o);
    }
  } else if (b < PB_W13) {
    const int rb = (b - PB_ST) * 16 + wv * 4;
    u32x4 v[4];
#pragma unroll
    for (int i = 0; i < 4; ++i) v[i] = *(const u32x4*)(x0 + (size_t)(rb + i) * D_ + lane * 8);
    float f[4][8], s[4], q[4];
#pragma unroll
    for (int i = 0; i < 4; ++i) {
#pragma unroll
      for (int j = 0; j < 4; ++j) { f[i][2 * j] = bflo(v[i][j]); f[i][2 * j + 1] = bfhi(v[i][j]); }
      s[i] = ((f[i][0] + f[i][1]) + (f[i][2] + f[i][3])) + ((f[i][4] + f[i][5]) + (f[i][6] + f[i][7]));
    }
#pragma unroll
    for (int o = 32; o > 0; o >>= 1)
#pragma unroll
      for (int i = 0; i < 4; ++i) s[i] += __shfl_xor(s[i], o);
#pragma unroll
    for (int i = 0; i < 4; ++i) {
      s[i] *= (1.0f / D_);
      q[i] = 0.f;
#pragma unroll
      for (int j = 0; j < 8; ++j) { float dlt = f[i][j] - s[i]; q[i] = fmaf(dlt, dlt, q[i]); }
    }
#pragma unroll
    for (int o = 32; o > 0; o >>= 1)
#pragma unroll
      for (int i = 0; i < 4; ++i) q[i] += __shfl_xor(q[i], o);
    {
      const int i = lane & 3;
      float mean = i == 0 ? s[0] : (i == 1 ? s[1] : (i == 2 ? s[2] : s[3]));
      float var = (i == 0 ? q[0] : (i == 1 ? q[1] : (i == 2 ? q[2] : q[3]))) * (1.0f / D_);
      float m, sd;
      ln_final(mean, var, m, sd);
      if (lane < 4) ((float2*)(ws + OFF_ST0))[rb + i] = make_float2(m, sd);
    }
  } else if (b < PB_W2) {
    const int bb = b - PB_W13;
    const int ft = bb >> 3, kp = bb & 7;
    u16* sm = (u16*)smem;
    constexpr int ST = 98;
    u32x4 v[6];
#pragma unroll
    for (int it = 0; it < 6; ++it) {
      int idx = tid + it * 256;
      int mat = idx / 768, rem = idx % 768;
      int r = rem / 12, c = (rem % 12) * 8;
      int f = ft * 96 + c;
      v[it] = (u32x4){0u, 0u, 0u, 0u};
      if (f < F_) v[it] = *(const u32x4*)((mat ? w3 : w1) + (size_t)(kp * 64 + r) * F_ + f);
    }
#pragma unroll
    for (int it = 0; it < 6; ++it) {
      int idx = tid + it * 256;
      int mat = idx / 768, rem = idx % 768;
      int r = rem / 12, c = (rem % 12) * 8;
#pragma unroll
      for (int i = 0; i < 4; ++i) *(u32*)(sm + (mat * 64 + r) * ST + c + 2 * i) = v[it][i];
    }
    __syncthreads();
    char* wo = ws + OFF_W13;
#pragma unroll
    for (int it = 0; it < 6; ++it) {
      int idx = tid + it * 256;
      int fr = idx >> 6, ln = idx & 63;
      int rt = fr >> 1, kk = fr & 1;
      int row = ln & 15, g = ln >> 4;
      int mat = (row >> 1) & 1;
      int fl = 8 * rt + 2 * (row >> 2) + (row & 1);
      int dl = 32 * kk + 8 * g;
      u32x4 o;
#pragma unroll
      for (int i = 0; i < 4; ++i)
        o[i] = (u32)sm[(mat * 64 + dl + 2 * i) * ST + fl] |
               ((u32)sm[(mat * 64 + dl + 2 * i + 1) * ST + fl] << 16);
      const int w = rt / 3, i3 = rt % 3;
      st16_wt(wo + ((size_t)((ft * 4 + w) * 16 + kp * 2 + kk) * 3 + i3) * 1024 + ln * 16, o);
    }
  } else if (b < PB_CS) {
    const int bb = b - PB_W2;
    const int ks = bb >> 1, hd = bb & 1;
    u16* sm = (u16*)smem;
    constexpr int ST = 258;
    u32x4 v[4];
#pragma unroll
    for (int it = 0; it < 4; ++it) {
      int idx = tid + it * 256;
      int r = idx >> 5, c = (idx & 31) * 8;
      int f = ks * 32 + r;
      v[it] = (u32x4){0u, 0u, 0u, 0u};
      if (f < F_) v[it] = *(const u32x4*)(w2 + (size_t)f * D_ + hd * 256 + c);
    }
#pragma unroll
    for (int it = 0; it < 4; ++it) {
      int idx = tid + it * 256;
      int r = idx >> 5, c = (idx & 31) * 8;
#pragma unroll
      for (int i = 0; i < 4; ++i) *(u32*)(sm + r * ST + c + 2 * i) = v[it][i];
    }
    __syncthreads();
    char* wo = ws + OFF_W2;
#pragma unroll
    for (int it = 0; it < 4; ++it) {
      int idx = tid + it * 256;
      int dt = idx >> 6, ln = idx & 63;
      int dd = 16 * dt + (ln & 15), g = ln >> 4;
      u32x4 o;
#pragma unroll
      for (int rt = 0; rt < 4; ++rt)
        o[rt] = (u32)sm[(8 * rt + 2 * g) * ST + dd] | ((u32)sm[(8 * rt + 2 * g + 1) * ST + dd] << 16);
      st16_wt(wo + ((size_t)ks * 32 + hd * 16 + dt) * 1024 + ln * 16, o);
    }
  } else {
    const int bb = b - PB_CS;
    const int mat = (tid >> 2) & 1, fq = tid & 3, rg = tid >> 3;
    const int f = bb * 32 + 8 * fq;
    float s[8];
#pragma unroll
    for (int j = 0; j < 8; ++j) s[j] = 0.f;
    if (f < F_) {
      const u16* w = (mat ? w3 : w1) + (size_t)(rg * 16) * F_ + f;
      u32x4 v[16];
#pragma unroll
      for (int r = 0; r < 16; ++r) v[r] = *(const u32x4*)(w + (size_t)r * F_);
#pragma unroll
      for (int r = 0; r < 16; ++r)
#pragma unroll
        for (int j = 0; j < 4; ++j) { s[2 * j] += bflo(v[r][j]); s[2 * j + 1] += bfhi(v[r][j]); }
    }
    float* sm = (float*)smem;
#pragma unroll
    for (int j = 0; j < 8; ++j) sm[rg * 65 + (tid & 7) * 8 + j] = s[j];
    __syncthreads();
    if (tid < 64) {
      float t = 0.f;
#pragma unroll
      for (int r = 0; r < 32; ++r) t += sm[r * 65 + tid];
      int m2 = tid >> 5, fo = tid & 31;
      int ff = bb * 32 + fo;
      int R = ff >> 3, fi = ff & 7;
      int row = 4 * (fi >> 1) + 2 * m2 + (fi & 1);
      ((float*)(ws + OFF_CS))[R * 16 + row] = t;
    }
  }
  STAMP(5); STAMP_END();
}

constexpr int KC_COPY = 4160;
constexpr int KC_CH = 4 * KC_COPY;
constexpr int CV_UR = 4608;
constexpr int CV_XS = 8192;
template <int NPART>
__global__ __launch_bounds__(512) void conv_kernel(const u16* __restrict__ xT,
                                                   const float* __restrict__ stats,
                                                   const char* __restrict__ KC,
                                                   const float* __restrict__ kinv,
                                                   const u16* __restrict__ Dskip,
                                                   const char* __restrict__ W13,
                                                   u16* __restrict__ xoT DIAG_ARG) {
  STAMP_BEGIN();
  __shared__ __attribute__((aligned(16))) char smem[2 * KC_CH + 2 * CV_UR + 2 * CV_XS + 4 * 4096];
  char* s_kc = smem;
  char* s_ur = smem + 2 * KC_CH;
  char* s_xs = s_ur + 2 * CV_UR;
  char* s_red = s_xs + 2 * CV_XS;
  const int tid = threadIdx.x, lane = tid & 63;
  const int wv = __builtin_amdgcn_readfirstlane(tid >> 6);
  const int d0 = chan_of_block(blockIdx.x);
  u32x2 xv[2];
#pragma unroll
  for (int c = 0; c < 2; ++c) xv[c] = *(const u32x2*)(xT + (size_t)(d0 + c) * L_ + 4 * tid);
  f32x4 pv[NPART][2];
#pragma unroll
  for (int p = 0; p < NPART; ++p) {
    const f32x4* sp = (const f32x4*)(stats + (size_t)p * L_ * 2 + 8 * tid);
    pv[p][0] = sp[0];
    pv[p][1] = sp[1];
  }
  const u32 dsk2 = *(const u32*)(Dskip + d0);
  const float ki = kinv[d0 + ((wv >> 1) & 1)];
  {
    const int c = tid >> 8, p = tid & 255;
    const char* src = KC + (size_t)(d0 + c) * KC_ROW + 32 + p * 16;
    u32x4 a = *(const u32x4*)(src);
    u32x2 pr = *(const u32x2*)(src - 8);
    u32 w0 = __builtin_amdgcn_alignbit(pr[1], pr[0], 16), w1 = __builtin_amdgcn_alignbit(a[0], pr[1], 16);
    u32 w2 = __builtin_amdgcn_alignbit(a[1], a[0], 16), w3 = __builtin_amdgcn_alignbit(a[2], a[1], 16);
    u32 w4 = __builtin_amdgcn_alignbit(a[3], a[2], 16);
    char* dst = s_kc + c * KC_CH + 32 + p * 16;
    *(u32x4*)(dst) = a;
    *(u32x4*)(dst + KC_COPY) = (u32x4){w1, w2, w3, w4};
    *(u32x4*)(dst + 2 * KC_COPY) = (u32x4){pr[1], a[0], a[1], a[2]};
    *(u32x4*)(dst + 3 * KC_COPY) = (u32x4){w0, w1, w2, w3};
  }
  float m[4], s[4];
  if (NPART == 1) {
#pragma unroll
    for (int i = 0; i < 4; ++i) { m[i] = pv[0][i >> 1][2 * (i & 1)]; s[i] = pv[0][i >> 1][2 * (i & 1) + 1]; }
  } else {
#pragma unroll
    for (int i = 0; i < 4; ++i) {
      float mean = 0.f;
#pragma unroll
      for (int p = 0; p < NPART; ++p) mean += pv[p][i >> 1][2 * (i & 1)];
      mean *= (1.0f / NPART);
      float M2 = 0.f, dv = 0.f;
#pragma unroll
      for (int p = 0; p < NPART; ++p) {
        M2 += pv[p][i >> 1][2 * (i & 1) + 1];
        float dl = pv[p][i >> 1][2 * (i & 1)] - mean;
        dv = fmaf(dl, dl, dv);
      }
      float var = (M2 + dv * (float)(D_ / NPART)) * (1.0f / D_);
      ln_final(mean, var, m[i], s[i]);
    }
  }
  STAMP(2);
  float rs[4];
#pragma unroll
  for (int i = 0; i < 4; ++i) rs[i] = __builtin_amdgcn_rcpf(s[i]);
#pragma unroll
  for (int c = 0; c < 2; ++c) {
    const float dsk = c ? bfhi(dsk2) : bflo(dsk2);
    float u[4];
    u32x4 xs;
#pragma unroll
    for (int i = 0; i < 4; ++i) {
      float x = (i & 1) ? bfhi(xv[c][i >> 1]) : bflo(xv[c][i >> 1]);
      float a = rbf(x - m[i]);
      float q = a * rs[i];
      float r = fmaf(-q, s[i], a);
      q = fmaf(r, rs[i], q);
      u[i] = rbf(q);
      xs[i] = pk_bf16(x, u[i] * dsk);
    }
    u32x2 ur = {pk_f16(u[3], u[2]), pk_f16(u[1], u[0])};
    *(u32x2*)(s_ur + c * CV_UR + 4088 - 8 * tid) = ur;
    *(u32x4*)(s_xs + c * CV_XS + tid * 16) = xs;
  }
  if (tid < 80) {
    u32x4 z = {0u, 0u, 0u, 0u};
    char* zp = tid < 64 ? s_ur + (tid >> 5) * CV_UR + (256 + (tid & 31)) * 16
                        : s_kc + ((tid - 64) >> 1) * KC_COPY + (tid & 1) * 16;
    *(u32x4*)zp = z;
  }
  STAMP(3);
  __syncthreads();
  STAMP(4);
  if (wv >= 4) {
    const int hf = blockIdx.x & 1, idx = blockIdx.x >> 3;
    const char* src = W13 + (size_t)hf * 7 * 196608 + (size_t)(idx * 4 + (wv - 4)) * 12288 + lane * 16;
    u32x4 dummy = {0u, 0u, 0u, 0u};
#pragma unroll
    for (int i = 0; i < 12; ++i)
      asm volatile("global_load_dwordx4 %0, %1, off" : "+v"(dummy) : "v"(src + i * 1024) : "memory");
    __builtin_amdgcn_s_barrier();
    asm volatile("s_waitcnt vmcnt(0)" : "+v"(dummy)::"memory");
    return;
  }
  const int ch = wv >> 1, hw = wv & 1;
  const int r16 = lane & 15, g = lane >> 4;
  const int aoff = ch * KC_CH + (3 - (r16 & 3)) * KC_COPY + 2 * (8 * g + 4 * ((r16 >> 2) + 1)) + 256 * hw;
  int aoff2 = aoff + 8;
  asm volatile("" : "+v"(aoff2));
  const char* abase = s_kc + aoff;
  const char* abase2 = s_kc + aoff2;
  const char* bbase = s_ur + ch * CV_UR + 2 * (2032 - 16 * r16 + 8 * g) - 512 * 7 + 256 * hw;
  f32x4 acc[8];
#pragma unroll
  for (int j = 0; j < 8; ++j) acc[j] = (f32x4){0.f, 0.f, 0.f, 0.f};
  f16x8 bf[2][8], af[2][8];
  auto ldA = [&](int buf, int wi, int cp) {
    u32x2 lo = *(const u32x2*)(abase + 64 * (8 * cp + wi));
    u32x2 hi = *(const u32x2*)(abase2 + 64 * (8 * cp + wi));
    u32x4 v = {lo[0], lo[1], hi[0], hi[1]};
    af[buf][cp] = __builtin_bit_cast(f16x8, v);
  };
  auto ldB = [&](int buf, int wi, int dl) { bf[buf][dl] = *(const f16x8*)(bbase + 512 * (7 - dl) + 64 * wi); };
#pragma unroll
  for (int j = 0; j < 8; ++j) { ldA(0, 0, j); ldB(0, 0, j); SB0(); }
  static_for<0, 4>([&](auto WI) {
    constexpr int wi = WI;
    constexpr int cur = wi & 1, nxt = cur ^ 1;
    static_for<0, 8>([&](auto MX) {
      constexpr int mx = MX;
      SB0();
      if constexpr (wi < 3) { ldA(nxt, wi + 1, mx); ldB(nxt, wi + 1, mx); }
      static_for<0, mx + 1>([&](auto O) {
        constexpr int o = O;
        if constexpr (mx + o < 8) {
          acc[mx + o] = __builtin_amdgcn_mfma_f32_16x16x32_f16(af[cur][mx], bf[cur][o], acc[mx + o], 0, 0, 0);
          if constexpr (o < mx)
            acc[mx + o] = __builtin_amdgcn_mfma_f32_16x16x32_f16(af[cur][o], bf[cur][mx], acc[mx + o], 0, 0, 0);
        }
      });
    });
  });
  SB0();
  STAMP(5);
  f32x4 keep[4];
  {
    f32x4* rp = (f32x4*)(s_red + wv * 4096) + lane;
#pragma unroll
    for (int j = 0; j < 4; ++j) {
      f32x4 snd;
#pragma unroll
      for (int r = 0; r < 4; ++r) {
        snd[r] = hw ? acc[j][r] : acc[4 + j][r];
        keep[j][r] = hw ? acc[4 + j][r] : acc[j][r];
      }
      rp[j * 64] = snd;
    }
  }
  raw_barrier();
  {
    const f32x4* rp = (const f32x4*)(s_red + (wv ^ 1) * 4096) + lane;
#pragma unroll
    for (int jj = 0; jj < 4; ++jj) {
      f32x4 o = rp[jj * 64];
      f32x4 a = keep[jj];
      const int t0 = 256 * (4 * hw + jj) + 16 * r16 + 4 * g;
      u32x4 xs = *(const u32x4*)(s_xs + ch * CV_XS + t0 * 4);
      float res[4];
#pragma unroll
      for (int r = 0; r < 4; ++r) {
        float y = rbf((a[r] + o[r]) * ki);
        float z = rbf(y + bfhi(xs[r]));
        res[r] = bflo(xs[r]) + z;
      }
      u32x2 ov = {pk_bf16(res[0], res[1]), pk_bf16(res[2], res[3])};
      *(u32x2*)(xoT + (size_t)(d0 + ch) * L_ + t0) = ov;
    }
  }
  STAMP(6); STAMP_END();
}

constexpr int g1_idx_x(int k, int j) { return k <= 1 ? 2 * k + j : 7 + 5 * (k - 2) + j; }
constexpr int g1_idx_w(int k, int i) { return k == 0 ? 4 + i : (k == 15 ? 77 + i : 7 + 5 * (k - 1) + 2 + i); }
constexpr int g1_issued(int ks) { return 22 + 5 * ks > 80 ? 80 : 22 + 5 * ks; }

__global__ __launch_bounds__(256, 1) void gemm1_kernel(const u16* __restrict__ xT,
                                                       const char* __restrict__ W13,
                                                       const float* __restrict__ cs,
                                                       const char* __restrict__ W2,
                                                       char* __restrict__ H DIAG_ARG) {
  STAMP_BEGIN();
  __shared__ __attribute__((aligned(16))) char smem[512 * 256 + 128 * 16];
  f32x4* s_st = (f32x4*)(smem + 512 * 256);
  const int tid = threadIdx.x, lane = tid & 63;
  const int wv = __builtin_amdgcn_readfirstlane(tid >> 6);
  const int hf = blockIdx.x & 1, lq = (blockIdx.x >> 1) & 3, idx = blockIdx.x >> 3;
  if (idx >= 30) {
    const char* src = W2 + (size_t)((idx - 30) * 22 * 32 + hf * 16 + wv * 4) * 1024 + lane * 16;
    u32x4 dummy = {0u, 0u, 0u, 0u};
    for (int ks = 0; ks < 22; ++ks) {
#pragma unroll
      for (int i = 0; i < 4; ++i)
        asm volatile("global_load_dwordx4 %0, %1, off" : "+v"(dummy) : "v"(src + ks * 32768 + i * 1024) : "memory");
    }
    asm volatile("s_waitcnt vmcnt(0)" : "+v"(dummy)::"memory");
    return;
  }
  const int mt = idx < 28 ? 4 * lq + (idx & 3) : 4 * lq + 2 * hf + (idx - 28);
  const int ft = idx < 28 ? 8 * hf + (idx >> 2) : 7;
  const int r16 = lane & 15, g = lane >> 4;
  const u32 lds0 = (u32)(uintptr_t)(LDS_AS char*)smem + wv * 2048;
  const char* xbase = (const char*)xT + (size_t)(8 * wv) * (L_ * 2) + mt * 256;
  const u32 xvoff = g * (L_ * 2) + (((r16 >> 1) ^ (g | ((wv & 1) << 2))) * 32) + (r16 & 1) * 16;
  const char* wbase = W13 + (size_t)(ft * 4 + wv) * 49152;
  const u32 wvoff = lane * 16;
  bf16x8 wf[16][3];
  auto ld_x = [&](auto K, auto J) {
    constexpr int k = K, j = J;
    glds16<(32 * k + 4 * j) * 256>(xvoff, lds0, xbase + (size_t)(32 * k + 4 * j) * (L_ * 2));
  };
  auto ld_w = [&](auto K, auto I) {
    constexpr int k = K, i = I;
    gld16<i * 1024>(wf[k][i], wvoff, wbase + k * 3072);
  };
  using I0 = std::integral_constant<int, 0>;
  using I1 = std::integral_constant<int, 1>;
  using I2 = std::integral_constant<int, 2>;
  ld_x(I0{}, I0{}); ld_x(I0{}, I1{}); ld_x(I1{}, I0{}); ld_x(I1{}, I1{});
  ld_w(I0{}, I0{}); ld_w(I0{}, I1{}); ld_w(I0{}, I2{});
  static_for<1, 4>([&](auto K) {
    constexpr int k = K;
    using KX = std::integral_constant<int, k + 1>;
    ld_x(KX{}, I0{}); ld_x(KX{}, I1{});
    ld_w(K, I0{}); ld_w(K, I1{}); ld_w(K, I2{});
  });
  const int q = (lane & 15) >> 2, p = lane & 3;
  const int hl = q | ((g & 1) << 2);
  int raddr[8];
#pragma unroll
  for (int j = 0; j < 8; ++j) raddr[j] = (8 * g + q) * 256 + 8 * p + ((((j + 2 * wv) & 7) ^ hl) * 32);
  f32x4 acc[3][8];
#pragma unroll
  for (int i = 0; i < 3; ++i)
#pragma unroll
    for (int lt = 0; lt < 8; ++lt) acc[i][lt] = (f32x4){0.f, 0.f, 0.f, 0.f};
  f32x4 sq[2], sm[2];
#pragma unroll
  for (int e = 0; e < 2; ++e) { sq[e] = (f32x4){0.f, 0.f, 0.f, 0.f}; sm[e] = (f32x4){0.f, 0.f, 0.f, 0.f}; }
  const u32x4 onesu = {0x3f803f80u, 0x3f803f80u, 0x3f803f80u, 0x3f803f80u};
  const bf16x8 ones = __builtin_bit_cast(bf16x8, onesu);
  u32x2 ulo[2][8], uhi[2][8];
  auto rd_lo = [&](int buf, int ks, int lt) {
    ulo[buf][lt] = __builtin_bit_cast(u32x2, __builtin_amdgcn_ds_read_tr16_b64_v4i16(
        (LDS_AS s16x4*)(smem + raddr[lt] + (32 * ks) * 256)));
  };
  auto rd_hi = [&](int buf, int ks, int lt) {
    uhi[buf][lt] = __builtin_bit_cast(u32x2, __builtin_amdgcn_ds_read_tr16_b64_v4i16(
        (LDS_AS s16x4*)(smem + raddr[lt] + (32 * ks + 4) * 256)));
  };
  auto uf = [&](int buf, int lt) {
    u32x4 v = {ulo[buf][lt][0], ulo[buf][lt][1], uhi[buf][lt][0], uhi[buf][lt][1]};
    return __builtin_bit_cast(bf16x8, v);
  };
  STAMP(2);
  wait_vm<g1_issued(0) - 4>();
  __builtin_amdgcn_s_barrier();
  asm volatile("" ::: "memory");
  STAMP(3);
#pragma unroll
  for (int lt = 0; lt < 8; ++lt) { rd_lo(0, 0, lt); rd_hi(0, 0, lt); }
  static_for<0, 16>([&](auto KS) {
    constexpr int ks = KS;
    constexpr int cur = ks & 1, nxt = cur ^ 1;
    SB0();
    wait_vm3<g1_issued(ks) - g1_idx_w(ks, 2) - 1>(wf[ks][0], wf[ks][1], wf[ks][2]);
    SB0();
    static_for<0, 8>([&](auto LT) {
      constexpr int lt = LT;
      acc[0][lt] = __builtin_amdgcn_mfma_f32_16x16x32_bf16(wf[ks][0], uf(cur, lt), acc[0][lt], 0, 0, 0);
      SB0();
      if constexpr (lt == 1 && (ks & 1) && ks < 15) {
        wait_vm<g1_issued(ks) - g1_idx_x(ks + 2 > 15 ? 15 : ks + 2, 1) - 1>();
        __builtin_amdgcn_s_barrier();
        asm volatile("" ::: "memory");
        SB0();
      }
      if constexpr (ks < 15 && lt >= 1) { rd_lo(nxt, ks + 1, lt - 1); rd_hi(nxt, ks + 1, lt - 1); }
      acc[1][lt] = __builtin_amdgcn_mfma_f32_16x16x32_bf16(wf[ks][1], uf(cur, lt), acc[1][lt], 0, 0, 0);
      SB0();
      if constexpr (lt >= 2 && lt < 4 && ks + 5 <= 15) ld_x(std::integral_constant<int, (ks + 5 <= 15 ? ks + 5 : 15)>{}, std::integral_constant<int, lt - 2>{});
      if constexpr (lt >= 4 && lt < 7 && ks + 4 <= 15) ld_w(std::integral_constant<int, (ks + 4 <= 15 ? ks + 4 : 15)>{}, std::integral_constant<int, lt - 4>{});
      if constexpr (ks < 15 && lt == 7) { rd_lo(nxt, ks + 1, 7); rd_hi(nxt, ks + 1, 7); }
      acc[2][lt] = __builtin_amdgcn_mfma_f32_16x16x32_bf16(wf[ks][2], uf(cur, lt), acc[2][lt], 0, 0, 0);
      SB0();
      if constexpr (lt < 2) {
        sq[lt] = __builtin_amdgcn_mfma_f32_16x16x32_bf16(uf(cur, lt), uf(cur, lt), sq[lt], 0, 0, 0);
        sm[lt] = __builtin_amdgcn_mfma_f32_16x16x32_bf16(ones, uf(cur, lt), sm[lt], 0, 0, 0);
        SB0();
      }
    });
  });
  f32x4 c4[3];
#pragma unroll
  for (int i = 0; i < 3; ++i) c4[i] = *(const f32x4*)(cs + (ft * 12 + 3 * wv + i) * 16 + 4 * g);
#pragma unroll
  for (int e = 0; e < 2; ++e) {
    const int r = r16 & 3;
    float s2 = r == 0 ? sq[e][0] : (r == 1 ? sq[e][1] : (r == 2 ? sq[e][2] : sq[e][3]));
    float mean = sm[e][0] * (1.0f / D_);
    float var = fmaxf(s2 * (1.0f / D_) - mean * mean, 0.f);
    float m, sd;
    ln_final(mean, var, m, sd);
    if (g == (r16 >> 2)) s_st[(2 * wv + e) * 16 + r16] = (f32x4){-m, -1.44269504f / sd, sd * sd, 0.f};
  }
  raw_barrier();
  u32* stg = (u32*)smem;
#pragma unroll
  for (int j = 0; j < 8; ++j) {
    const int lt = (j + 2 * wv) & 7;
    const f32x4 st = s_st[lt * 16 + r16];
#pragma unroll
    for (int i = 0; i < 3; ++i) {
      const int R = 3 * wv + i;
      f32x2 a = {fmaf(st[0], c4[i][0], acc[i][j][0]), fmaf(st[0], c4[i][1], acc[i][j][1])};
      f32x2 b2 = {fmaf(st[0], c4[i][2], acc[i][j][2]), fmaf(st[0], c4[i][3], acc[i][j][3])};
      f32x2 t = a * st[1];
      f32x2 ex = {__builtin_amdgcn_exp2f(t[0]), __builtin_amdgcn_exp2f(t[1])};
      f32x2 den = ex * st[2] + st[2];
      f32x2 rc = {__builtin_amdgcn_rcpf(den[0]), __builtin_amdgcn_rcpf(den[1])};
      f32x2 hv = (a * b2) * rc;
      stg[(((R >> 2) * 8 + lt) * 4 + (R & 3)) * 64 + lane] = pk_bf16(hv[0], hv[1]);
    }
  }
  STAMP(8);
  raw_barrier();
#pragma unroll
  for (int it = 0; it < 6; ++it) {
    const int pc = wv * 6 + it;
    u32x4 o;
#pragma unroll
    for (int rt = 0; rt < 4; ++rt) o[rt] = stg[(pc * 4 + rt) * 64 + lane];
    *(u32x4*)(H + ((size_t)(ft * 3 + (pc >> 3)) * 128 + mt * 8 + (pc & 7)) * 1024 + lane * 16) = o;
  }
  STAMP(9); STAMP_END();
}

template <int FINAL>
__global__ __launch_bounds__(256, 1) void gemm2_kernel(const char* __restrict__ H,
                                                       const char* __restrict__ W2,
                                                       const u16* __restrict__ xT,
                                                       u16* __restrict__ xoT,
                                                       float* __restrict__ stats,
                                                       u16* __restrict__ out DIAG_ARG) {
  STAMP_BEGIN();
  __shared__ __attribute__((aligned(16))) char smem[4 * 12 * 1024 + 4 * 16 * 144];
  const int tid = threadIdx.x, lane = tid & 63, wv = tid >> 6;
  const int hf = blockIdx.x & 1, lq = (blockIdx.x >> 1) & 3, idx = blockIdx.x >> 3;
  const int mt = 8 * lq + (idx & 7), nt = 4 * hf + (idx >> 3);
  const int r16 = lane & 15, g = lane >> 4;
  const char* hsrc = H + ((size_t)wv * 128 + 4 * mt) * 1024 + lane * 16;
  const char* wsrc = W2 + ((size_t)wv * 32 + 4 * nt) * 1024 + lane * 16;
  constexpr int NS = 11, PF = 3;
  bf16x8 hf_[NS][4], wf[NS][4];
  auto ld = [&](int s, int j) {
    if (j & 1) wf[s][j >> 1] = *(const bf16x8*)(wsrc + ((size_t)s * 4 * 32 + (j >> 1)) * 1024);
    else hf_[s][j >> 1] = *(const bf16x8*)(hsrc + ((size_t)s * 4 * 128 + (j >> 1)) * 1024);
  };
#pragma unroll
  for (int s = 0; s < PF; ++s) {
#pragma unroll
    for (int j = 0; j < 8; ++j) ld(s, j);
    SB0();
  }
  const int lrow = 64 * mt + 16 * wv + 4 * g;
  u32x2 xres[4];
  STAMP(2);
  f32x4 acc[4][4];
#pragma unroll
  for (int a = 0; a < 4; ++a)
#pragma unroll
    for (int b2 = 0; b2 < 4; ++b2) acc[a][b2] = (f32x4){0.f, 0.f, 0.f, 0.f};
#pragma unroll
  for (int s = 0; s < NS; ++s) {
#pragma unroll
    for (int j = 0; j < 8; ++j) {
      SB0();
      constexpr int oa[16] = {0, 0, 1, 1, 0, 1, 2, 2, 2, 0, 1, 2, 3, 3, 3, 3};
      constexpr int ob[16] = {0, 1, 0, 1, 2, 2, 0, 1, 2, 3, 3, 3, 0, 1, 2, 3};
#pragma unroll
      for (int e = 0; e < 2; ++e) {
        const int a = oa[2 * j + e], b2 = ob[2 * j + e];
        acc[a][b2] = __builtin_amdgcn_mfma_f32_16x16x32_bf16(hf_[s][a], wf[s][b2], acc[a][b2], 0, 0, 0);
      }
      SB0();
      if (s + PF < NS) ld(s + PF, j);
      else if (s + PF == NS && j < 4)
        xres[j] = *(const u32x2*)(xT + (size_t)(64 * nt + 16 * j + r16) * L_ + lrow);
    }
  }
  SB0();
  STAMP(3);
  f32x4 own[4];
#pragma unroll
  for (int a = 0; a < 4; ++a) {
    if (a == wv) {
#pragma unroll
      for (int b2 = 0; b2 < 4; ++b2) own[b2] = acc[a][b2];
    } else {
      const int src = (wv - a - 1) & 3;
      f32x4* dst = (f32x4*)(smem + (a * 12 + src * 4) * 1024) + lane;
#pragma unroll
      for (int b2 = 0; b2 < 4; ++b2) dst[b2 * 64] = acc[a][b2];
    }
  }
  __syncthreads();
  {
    const f32x4* sp = (const f32x4*)(smem + wv * 12 * 1024) + lane;
#pragma unroll
    for (int src = 0; src < 3; ++src)
#pragma unroll
      for (int b2 = 0; b2 < 4; ++b2) own[b2] += sp[(src * 4 + b2) * 64];
  }
  STAMP(4);
  float xo[4][4];
#pragma unroll
  for (int b2 = 0; b2 < 4; ++b2)
#pragma unroll
    for (int r = 0; r < 4; ++r) {
      float xr = (r & 1) ? bfhi(xres[b2][r >> 1]) : bflo(xres[b2][r >> 1]);
      xo[b2][r] = rbf(xr + rbf(own[b2][r]));
    }
  if (!FINAL) {
#pragma unroll
    for (int b2 = 0; b2 < 4; ++b2) {
      u32x2 ov = {pk_bf16(xo[b2][0], xo[b2][1]), pk_bf16(xo[b2][2], xo[b2][3])};
      *(u32x2*)(xoT + (size_t)(64 * nt + 16 * b2 + r16) * L_ + lrow) = ov;
    }
    float sm[4], q[4];
#pragma unroll
    for (int r = 0; r < 4; ++r) sm[r] = (xo[0][r] + xo[1][r]) + (xo[2][r] + xo[3][r]);
#pragma unroll
    for (int o = 8; o > 0; o >>= 1)
#pragma unroll
      for (int r = 0; r < 4; ++r) sm[r] += __shfl_xor(sm[r], o);
#pragma unroll
    for (int r = 0; r < 4; ++r) {
      sm[r] *= (1.0f / 64.0f);
      q[r] = 0.f;
#pragma unroll
      for (int b2 = 0; b2 < 4; ++b2) { float dl = xo[b2][r] - sm[r]; q[r] = fmaf(dl, dl, q[r]); }
    }
#pragma unroll
    for (int o = 8; o > 0; o >>= 1)
#pragma unroll
      for (int r = 0; r < 4; ++r) q[r] += __shfl_xor(q[r], o);
    if (r16 == 0) {
      f32x4* sp = (f32x4*)(stats + ((size_t)nt * L_ + lrow) * 2);
      sp[0] = (f32x4){sm[0], q[0], sm[1], q[1]};
      sp[1] = (f32x4){sm[2], q[2], sm[3], q[3]};
    }
  } else {
    char* tp = smem + 4 * 12 * 1024 + wv * 16 * 144;
#pragma unroll
    for (int b2 = 0; b2 < 4; ++b2)
#pragma unroll
      for (int r = 0; r < 4; ++r)
        *(u16*)(tp + (4 * g + r) * 144 + (16 * b2 + r16) * 2) = (u16)(__float_as_uint(xo[b2][r]) >> 16);
    wave_fence();
#pragma unroll
    for (int it = 0; it < 2; ++it) {
      const int row = (lane >> 3) + 8 * it, cc = lane & 7;
      u32x4 v = *(const u32x4*)(tp + row * 144 + cc * 16);
      *(u32x4*)(out + (size_t)(64 * mt + 16 * wv + row) * D_ + 64 * nt + cc * 8) = v;
    }
  }
  STAMP(5); STAMP_END();
}

extern "C" void kernel_launch(void* const* d_in, const int* in_sizes, int n_in, void* d_out,
                              int out_size, void* d_ws, size_t ws_size, hipStream_t stream) {
  const u16* x0 = (const u16*)d_in[0];
  const u16* logA = (const u16*)d_in[1];
  const u16* Aim = (const u16*)d_in[2];
  const u16* Cre = (const u16*)d_in[3];
  const u16* Cim = (const u16*)d_in[4];
  const u16* logdt = (const u16*)d_in[5];
  const u16* Dsk = (const u16*)d_in[6];
  const u16* w1 = (const u16*)d_in[8];
  const u16* w3 = (const u16*)d_in[9];
  const u16* w2 = (const u16*)d_in[10];
  char* ws = (char*)d_ws;
  u16* out = (u16*)d_out;
  prep_kernel<<<PB_END, 256, 0, stream>>>(x0, logA, Aim, Cre, Cim, logdt, w1, w3, w2, ws DIAG_PASS(0));
  conv_kernel<1><<<256, 512, 0, stream>>>((const u16*)(ws + OFF_XT0), (const float*)(ws + OFF_ST0),
                                          ws + OFF_KC, (const float*)(ws + OFF_KINV), Dsk, ws + OFF_W13,
                                          (u16*)(ws + OFF_X1T) DIAG_PASS(1));
  gemm1_kernel<<<256, 256, 0, stream>>>((const u16*)(ws + OFF_X1T), ws + OFF_W13,
                                        (const float*)(ws + OFF_CS), ws + OFF_W2, ws + OFF_H DIAG_PASS(2));
  gemm2_kernel<0><<<256, 256, 0, stream>>>(ws + OFF_H, ws + OFF_W2, (const u16*)(ws + OFF_X1T),
                                           (u16*)(ws + OFF_X2T), (float*)(ws + OFF_ST2), out DIAG_PASS(3));
  conv_kernel<8><<<256, 512, 0, stream>>>((const u16*)(ws + OFF_X2T), (const float*)(ws + OFF_ST2),
                                          ws + OFF_KC, (const float*)(ws + OFF_KINV), Dsk, ws + OFF_W13,
                                          (u16*)(ws + OFF_X3T) DIAG_PASS(4));
  gemm1_kernel<<<256, 256, 0, stream>>>((const u16*)(ws + OFF_X3T), ws + OFF_W13,
                                        (const float*)(ws + OFF_CS), ws + OFF_W2, ws + OFF_H DIAG_PASS(5));
  gemm2_kernel<1><<<256, 256, 0, stream>>>(ws + OFF_H, ws + OFF_W2, (const u16*)(ws + OFF_X3T),
                                           (u16*)(ws + OFF_X2T), (float*)(ws + OFF_ST2), out DIAG_PASS(6));
}
```

```cpp
#include <hip/hip_runtime.h>
#include <hip/hip_bf16.h>
#include <stdint.h>
#include <type_traits>

typedef unsigned short u16;
typedef unsigned int u32;
typedef float f32x4 __attribute__((ext_vector_type(4)));
typedef float f32x2 __attribute__((ext_vector_type(2)));
typedef __bf16 bf16x8 __attribute__((ext_vector_type(8)));
typedef __bf16 bf16x2 __attribute__((ext_vector_type(2)));
typedef _Float16 f16x8 __attribute__((ext_vector_type(8)));
typedef _Float16 f16x2 __attribute__((ext_vector_type(2)));
typedef short s16x4 __attribute__((ext_vector_type(4)));
typedef u32 u32x4 __attribute__((ext_vector_type(4)));
typedef u32 u32x3 __attribute__((ext_vector_type(3)));
typedef u32 u32x2 __attribute__((ext_vector_type(2)));
#define LDS_AS __attribute__((address_space(3)))
#define DEVI __device__ __forceinline__
#define SB0() __builtin_amdgcn_sched_barrier(0)
#define AI __attribute__((always_inline))
#if defined(__HIP_DEVICE_COMPILE__)
#define NOPK __attribute__((target("no-packed-fp32-ops")))
#else
#define NOPK
#endif
typedef double f64x2 __attribute__((ext_vector_type(2)));

#define DIAG_ARG
#define DIAG_PASS(k)
#define STAMP_BEGIN()
#define STAMP(k)
#define RSTAMP(k)
#define STAMP_END()

template <int I, int N, class F>
DEVI void static_for(F&& f) {
  if constexpr (I < N) {
    f(std::integral_constant<int, I>{});
    static_for<I + 1, N>(f);
  }
}

constexpr int L_ = 2048, D_ = 512, N_ = 128, F_ = 1376;
constexpr int FT = 86;
constexpr int NKS2 = 44;
constexpr int KC_ROW = 4160;
constexpr size_t XBYTES = (size_t)L_ * D_ * 2;

constexpr size_t OFF_X0 = 0;
constexpr size_t OFF_X1 = OFF_X0 + XBYTES;
constexpr size_t OFF_X2 = OFF_X1 + XBYTES;
constexpr size_t OFF_X3 = OFF_X2 + XBYTES;
constexpr size_t OFF_ST0 = OFF_X3 + XBYTES;
constexpr size_t OFF_ST2 = OFF_ST0 + (size_t)L_ * 16;
constexpr size_t OFF_FL = OFF_ST2 + (size_t)L_ * 16;
constexpr size_t OFF_KC = OFF_FL + 65536;
constexpr size_t OFF_KINV = OFF_KC + (size_t)D_ * KC_ROW;
constexpr size_t OFF_W13 = OFF_KINV + 2048;
constexpr size_t OFF_CS = OFF_W13 + (size_t)16 * 196608;
constexpr size_t OFF_W2 = OFF_CS + (size_t)8 * 16 * 176 * 4;
constexpr size_t OFF_H = OFF_W2 + (size_t)NKS2 * 32 * 1024;
constexpr size_t HBYTES = (size_t)NKS2 * 128 * 1024;
constexpr size_t WS_END = OFF_H + 2 * HBYTES;

DEVI float bf2f(u16 v) { return __uint_as_float(((u32)v) << 16); }
DEVI float bflo(u32 v) { return __uint_as_float(v << 16); }
DEVI float bfhi(u32 v) { return __uint_as_float(v & 0xffff0000u); }
DEVI u32 pk_bf16(float lo, float hi) {
  f32x2 v = {lo, hi};
  bf16x2 b = __builtin_convertvector(v, bf16x2);
  return __builtin_bit_cast(u32, b);
}
DEVI float rbf(float x) { return (float)((__bf16)x); }
DEVI u32 pk_f16(float lo, float hi) {
  f32x2 v = {lo, hi};
  f16x2 h = __builtin_convertvector(v, f16x2);
  return __builtin_bit_cast(u32, h);
}
template <int CTRL>
DEVI float dpp_add(float v) {
  return v + __builtin_bit_cast(float, __builtin_amdgcn_update_dpp(0, __builtin_bit_cast(int, v), CTRL, 0xf, 0xf, false));
}
DEVI float row16_sum(float v) {
  v = dpp_add<0x128>(v);
  v = dpp_add<0x124>(v);
  v = dpp_add<0x122>(v);
  v = dpp_add<0x121>(v);
  return v;
}
DEVI float oct_sum(float v) {
  v = dpp_add<0xB1>(v);
  v = dpp_add<0x4E>(v);
  v = dpp_add<0x141>(v);
  return v;
}
DEVI f32x2 rbf2(f32x2 v) {
  const u32 p = pk_bf16(v[0], v[1]);
  return (f32x2){bflo(p), bfhi(p)};
}
DEVI void ln_final2(f32x2 s1, f32x2 s2, f32x2& m, f32x2& s) {
  const f32x2 mean = s1 * (1.0f / D_);
  f32x2 var = __builtin_elementwise_fma(-mean, mean, s2 * (1.0f / D_));
  var = (f32x2){fmaxf(var[0], 0.f), fmaxf(var[1], 0.f)};
  m = rbf2(mean);
  const f32x2 ve = rbf2(rbf2(var) + 1e-5f);
  s = rbf2((f32x2){__builtin_amdgcn_sqrtf(ve[0]), __builtin_amdgcn_sqrtf(ve[1])});
}
DEVI void swap16(u32& x, u32& y) {
  auto r = __builtin_amdgcn_permlane16_swap(x, y, false, false);
  x = r[0]; y = r[1];
}
DEVI void swap32(u32& x, u32& y) {
  auto r = __builtin_amdgcn_permlane32_swap(x, y, false, false);
  x = r[0]; y = r[1];
}
DEVI void st16_wt(void* p, u32x4 v) {
  asm volatile("global_store_dwordx4 %0, %1, off sc1\n\ts_nop 1" ::"v"(p), "v"(v) : "memory");
}
DEVI void raw_barrier() {
  asm volatile("s_waitcnt lgkmcnt(0)" ::: "memory");
  __builtin_amdgcn_s_barrier();
  asm volatile("" ::: "memory");
}
DEVI void bare_barrier() {
  asm volatile("" ::: "memory");
  __builtin_amdgcn_s_barrier();
  asm volatile("" ::: "memory");
}
DEVI void glds16(u32 voff, u32 ldsaddr, const char* sbase) {
  asm volatile("s_mov_b32 m0, %1\n\ts_nop 0\n\tglobal_load_lds_dwordx4 %0, %2"
               ::"v"(voff), "s"(ldsaddr), "s"(sbase) : "memory");
}
template <int N>
DEVI void wait_vm() { asm volatile("s_waitcnt vmcnt(%0)" ::"i"(N) : "memory"); }
template <class T>
DEVI void tie(T& a) { asm volatile("" : "+v"(a)); }
template <class T>
DEVI void ties(T& a) { asm volatile("" : "+s"(a)); }
DEVI u32x2 tr_read(const char* p) {
  return __builtin_bit_cast(u32x2, __builtin_amdgcn_ds_read_tr16_b64_v4i16((LDS_AS s16x4*)p));
}
DEVI void wave_fence() {
  __builtin_amdgcn_fence(__ATOMIC_RELEASE, "wavefront");
  __builtin_amdgcn_wave_barrier();
  __builtin_amdgcn_fence(__ATOMIC_ACQUIRE, "wavefront");
}
DEVI void warm16(u32x4& dummy, const char* p) {
  asm volatile("global_load_dwordx4 %0, %1, off" : "+v"(dummy) : "v"(p) : "memory");
}
DEVI int chan_of_block(int b) { return 256 * (b & 1) + 64 * ((b >> 1) & 3) + 2 * (b >> 3); }

DEVI f32x2 cmul(f32x2 a, f32x2 b) {
  const f32x2 t = (f32x2){a[0], a[0]} * b;
  return __builtin_elementwise_fma((f32x2){-a[1], a[1]}, (f32x2){b[1], b[0]}, t);
}

constexpr int PB_X = 256, PB_W13 = 512, PB_W2 = 640, PB_ST = 728, PB_END = 856;
constexpr int KG_ROWB = 272;
constexpr int KG_BUF = 16 * KG_ROWB;
constexpr int KG_WAVE = 2 * KG_BUF;

__global__ __launch_bounds__(256, 4) void prep_kernel(
    const u16* __restrict__ x0, const u16* __restrict__ logA, const u16* __restrict__ Aim,
    const u16* __restrict__ Cre, const u16* __restrict__ Cim, const u16* __restrict__ logdt,
    const u16* __restrict__ Dskip,
    const u16* __restrict__ w1, const u16* __restrict__ w3, const u16* __restrict__ w2,
    char* __restrict__ ws DIAG_ARG) {
  STAMP_BEGIN();
  __shared__ __attribute__((aligned(16))) char smem[4 * KG_WAVE + 64];
  const int tid = threadIdx.x, lane = tid & 63, wv = tid >> 6;
  const int b = blockIdx.x;
  const int r16 = lane & 15, g = lane >> 4;
  const int tq = r16 >> 2, tp = lane & 3;
  if (b < PB_X) {
    const int ch = wv >> 1;
    const int dbase = chan_of_block(b);
    const int d = dbase + ch;
    const int n = 64 * (wv & 1) + lane;
    char* buf = smem + wv * KG_WAVE;
    float* red = (float*)(smem + 4 * KG_WAVE);
    const u16 i_dt = logdt[d], i_la = logA[d * N_ + n], i_ai = Aim[d * N_ + n], i_cr = Cre[d * N_ + n], i_ci = Cim[d * N_ + n];
    const u32 dsk2 = *(const u32*)(Dskip + dbase);
    const float dt = rbf(expf(bf2f(i_dt)));
    const float ea = rbf(expf(bf2f(i_la)));
    const float xl = (-ea * dt) * 1.44269504f;
    const float tr = (bf2f(i_ai) * dt) * 0.159154943f;
    f32x2 C = {2.0f * bf2f(i_cr) * dt, 2.0f * bf2f(i_ci) * dt};
    float isw;
    {
      float bs = row16_sum(fabsf(C[0]) + fabsf(C[1]));
      bs += __shfl_xor(bs, 16);
      bs += __shfl_xor(bs, 32);
      if (lane == 0) red[wv] = bs;
      int e = 0;
      if (bs > 0.f) {
        e = 14 - (((int)((__float_as_uint(bs) >> 23) & 0xff)) - 127);
        e = e > 100 ? 100 : (e < -100 ? -100 : e);
      }
      C *= __uint_as_float((u32)(127 + e) << 23);
      isw = __uint_as_float((u32)(127 - e) << 23);
    }
    auto cexp2k = [&](int k) {
      const float sc = (float)(1 << k);
      const float rk = __builtin_amdgcn_fractf(tr * sc);
      const float mag = __builtin_amdgcn_exp2f(xl * sc);
      return (f32x2){mag * __builtin_amdgcn_cosf(rk), mag * __builtin_amdgcn_sinf(rk)};
    };
    f32x2 Bk[12];
    Bk[0] = cexp2k(0); Bk[3] = cexp2k(3); Bk[7] = cexp2k(7); Bk[10] = cexp2k(10);
    Bk[3][1] = -Bk[3][1];
    Bk[1] = cmul(Bk[0], Bk[0]); Bk[4] = cmul(Bk[3], Bk[3]); Bk[8] = cmul(Bk[7], Bk[7]); Bk[11] = cmul(Bk[10], Bk[10]);
    Bk[2] = cmul(Bk[1], Bk[1]); Bk[5] = cmul(Bk[4], Bk[4]); Bk[9] = cmul(Bk[8], Bk[8]);
    Bk[6] = cmul(Bk[5], Bk[5]);
    STAMP(2);
    auto put = [&](int bf, int row, f32x2 v) {
      *(u32*)(buf + bf * KG_BUF + row * KG_ROWB + lane * 4) = pk_f16(v[0], v[1]);
    };
    auto frag = [&](int bf, int s) {
      return *(const f16x8*)(buf + bf * KG_BUF + r16 * KG_ROWB + (16 * s + 4 * g) * 4);
    };
    {
      f32x2 Q[16];
      Q[0] = (f32x2){1.f, 0.f};
      Q[1] = Bk[3]; Q[2] = Bk[4]; Q[3] = cmul(Q[1], Bk[4]);
      Q[4] = Bk[5]; Q[5] = cmul(Q[1], Bk[5]); Q[6] = cmul(Q[2], Bk[5]); Q[7] = cmul(Q[3], Bk[5]);
      Q[8] = Bk[6];
#pragma unroll
      for (int j = 1; j < 8; ++j) Q[8 + j] = cmul(Q[j], Bk[6]);
#pragma unroll
      for (int j = 0; j < 16; ++j) put(0, j, Q[j]);
    }
    f32x2 F[8];
    F[1] = Bk[0]; F[2] = Bk[1]; F[3] = cmul(Bk[0], Bk[1]);
    F[4] = Bk[2]; F[5] = cmul(F[1], Bk[2]); F[6] = cmul(F[2], Bk[2]); F[7] = cmul(F[3], Bk[2]);
    f32x2 CG[8];
    CG[0] = C;
    CG[1] = cmul(C, Bk[7]);
#pragma unroll
    for (int j1 = 0; j1 < 2; ++j1) {
      put(1, 8 * j1, CG[j1]);
#pragma unroll
      for (int bb = 1; bb < 8; ++bb) put(1, 8 * j1 + bb, cmul(CG[j1], F[bb]));
    }
    wave_fence();
    f16x8 bq[4];
    f32x4 tacc[3];
#pragma unroll
    for (int s = 0; s < 4; ++s) bq[s] = frag(0, s);
    tacc[0] = (f32x4){0.f, 0.f, 0.f, 0.f};
#pragma unroll
    for (int s = 0; s < 4; ++s) tacc[0] = __builtin_amdgcn_mfma_f32_16x16x32_f16(frag(1, s), bq[s], tacc[0], 0, 0, 0);
    wave_fence();
    CG[2] = cmul(C, Bk[8]); CG[3] = cmul(CG[1], Bk[8]);
#pragma unroll
    for (int j1 = 2; j1 < 4; ++j1) {
      put(0, 4 * (j1 - 2), CG[j1]);
#pragma unroll
      for (int bb = 1; bb < 4; ++bb) put(0, 4 * (j1 - 2) + bb, cmul(CG[j1], F[2 * bb]));
    }
#pragma unroll
    for (int j = 0; j < 4; ++j) {
      CG[4 + j] = cmul(CG[j], Bk[9]);
      put(0, 8 + 2 * j, CG[4 + j]);
      put(0, 9 + 2 * j, cmul(CG[4 + j], F[4]));
    }
#pragma unroll
    for (int j = 0; j < 8; ++j) put(1, j, cmul(CG[j], Bk[10]));
    put(1, 8, cmul(C, Bk[11]));
    wave_fence();
    tacc[1] = (f32x4){0.f, 0.f, 0.f, 0.f};
    tacc[2] = (f32x4){0.f, 0.f, 0.f, 0.f};
#pragma unroll
    for (int s = 0; s < 4; ++s) {
      tacc[1] = __builtin_amdgcn_mfma_f32_16x16x32_f16(frag(0, s), bq[s], tacc[1], 0, 0, 0);
      tacc[2] = __builtin_amdgcn_mfma_f32_16x16x32_f16(frag(1, s), bq[s], tacc[2], 0, 0, 0);
    }
    wave_fence();
    {
      float* tab = (float*)buf;
#pragma unroll
      for (int tile = 0; tile < 3; ++tile)
#pragma unroll
        for (int r = 0; r < 4; ++r) tab[(tile * 16 + 4 * g + r) * 16 + r16] = tacc[tile][r] * isw;
    }
    STAMP(3);
    __syncthreads();
    float scale[2];
#pragma unroll
    for (int c = 0; c < 2; ++c) {
      float mx = red[2 * c] + red[2 * c + 1] + fabsf(c ? bfhi(dsk2) : bflo(dsk2));
      int e = 0;
      if (mx > 0.f) {
        e = 14 - (((int)((__float_as_uint(mx) >> 23) & 0xff)) - 127);
        e = e > 100 ? 100 : (e < -100 ? -100 : e);
      }
      scale[c] = __uint_as_float((u32)(127 + e) << 23);
      if (tid == c) ((float*)(ws + OFF_KINV))[dbase + c] = __uint_as_float((u32)(127 - e) << 23);
    }
    auto tabv = [&](int c, int idx) {
      const float* t0 = (const float*)(smem + (2 * c) * KG_WAVE);
      const float* t1 = (const float*)(smem + (2 * c + 1) * KG_WAVE);
      return (t0[idx] + t1[idx]) * (c ? scale[1] : scale[0]);
    };
    auto first_idx = [&](int q) {
      int j1 = q >> 4, j0 = q & 15;
      int row = j1 < 2 ? 8 * j1 : (j1 < 4 ? 16 + 4 * (j1 - 2) : (j1 < 8 ? 24 + 2 * (j1 - 4) : 32 + (j1 - 8)));
      return row * 16 + j0;
    };
    auto kout = [&](int c, int q, u32x4 o) {
      *(u32x4*)(ws + OFF_KC + (size_t)(dbase + c) * KC_ROW + 32 + q * 16) = o;
    };
    if (wv == 0) {
      const int c = lane >> 5, q = lane & 31;
      const int i0 = first_idx(q);
      float v[8];
#pragma unroll
      for (int i = 0; i < 8; ++i) v[i] = tabv(c, i0 + 16 * i);
      if (q == 0) v[0] += c ? bfhi(dsk2) * scale[1] : bflo(dsk2) * scale[0];
      kout(c, q, (u32x4){pk_f16(v[0], v[1]), pk_f16(v[2], v[3]), pk_f16(v[4], v[5]), pk_f16(v[6], v[7])});
    } else if (wv == 1) {
      const int c = lane >> 5, q = 32 + (lane & 31);
      const int i0 = first_idx(q);
      float v0 = tabv(c, i0), v2 = tabv(c, i0 + 16), v4 = tabv(c, i0 + 32), v6 = tabv(c, i0 + 48);
      float v8 = tabv(c, first_idx(q + 1));
      kout(c, q, (u32x4){pk_f16(v0, v0), pk_f16(v2, v4), pk_f16(v4, v4), pk_f16(v6, v8)});
    } else {
      const int c = wv - 2, q = 64 + lane;
      const int i0 = first_idx(q);
      float v0 = tabv(c, i0), v4 = tabv(c, i0 + 16), v8 = tabv(c, first_idx(q + 1));
      kout(c, q, (u32x4){pk_f16(v0, v0), pk_f16(v0, v4), pk_f16(v4, v4), pk_f16(v8, v8)});
    }
    {
      const int c = wv >> 1, q = 128 + 64 * (wv & 1) + lane;
      float v0 = tabv(c, first_idx(q)), v8 = tabv(c, first_idx(q + 1));
      float vm = (q & 1) ? v8 : v0;
      kout(c, q, (u32x4){pk_f16(v0, v0), pk_f16(v0, v0), pk_f16(vm, v8), pk_f16(v8, v8)});
    }
    if (tid < 8) {
      const int c = tid >> 2, p = tid & 3;
      kout(c, p < 2 ? p - 2 : 254 + p, (u32x4){0u, 0u, 0u, 0u});
    }
  } else if (b < PB_W13) {
    const int tb = b - PB_X;
    const int l0 = (tb >> 3) * 64, nt = tb & 7;
    constexpr int ST = 144;
    u32x4 v[2];
#pragma unroll
    for (int it = 0; it < 2; ++it)
      v[it] = *(const u32x4*)(x0 + (size_t)(l0 + 32 * it + (tid >> 3)) * D_ + nt * 64 + (tid & 7) * 8);
#pragma unroll
    for (int it = 0; it < 2; ++it) *(u32x4*)(smem + (32 * it + (tid >> 3)) * ST + (tid & 7) * 16) = v[it];
    __syncthreads();
#pragma unroll
    for (int kk = 0; kk < 2; ++kk) {
      const char* src = smem + (32 * kk + 8 * g + tq) * ST + wv * 32 + tp * 8;
      u32x2 lo = tr_read(src), hi = tr_read(src + 4 * ST);
      st16_wt(ws + OFF_X0 + ((size_t)(nt * 64 + wv * 16 + r16) * L_ + l0 + 32 * kk + 8 * g) * 2,
              (u32x4){lo[0], lo[1], hi[0], hi[1]});
    }
  } else if (b < PB_W2) {
    const int bb = b - PB_W13;
    const int ft = bb >> 3, kp = bb & 7;
    constexpr int ST = 368;
    u32x4 va[3], vb[3];
#pragma unroll
    for (int it = 0; it < 3; ++it) {
      const int idx = tid + it * 256;
      const int r = idx / 11, gr = idx - r * 11;
      if (idx < 704) {
        const size_t o = (size_t)(kp * 64 + r) * F_ + ft * FT + gr * 8;
        const u32* pa = (const u32*)(w1 + o);
        const u32* pb = (const u32*)(w3 + o);
        va[it][0] = pa[0]; va[it][1] = pa[1]; va[it][2] = pa[2];
        vb[it][0] = pb[0]; vb[it][1] = pb[1]; vb[it][2] = pb[2];
        va[it][3] = 0x00003f80u; vb[it][3] = 0u;
        if (gr < 10) { va[it][3] = pa[3]; vb[it][3] = pb[3]; }
      }
    }
#pragma unroll
    for (int it = 0; it < 3; ++it) {
      const int idx = tid + it * 256;
      const int r = idx / 11, gr = idx - r * 11;
      if (idx < 704) {
        char* dst = smem + r * ST + gr * 32;
        *(u32x4*)dst = (u32x4){va[it][0], vb[it][0], va[it][1], vb[it][1]};
        *(u32x4*)(dst + 16) = (u32x4){va[it][2], vb[it][2], va[it][3], vb[it][3]};
      }
    }
    __syncthreads();
    const u32x4 onesu = {0x3f803f80u, 0x3f803f80u, 0x3f803f80u, 0x3f803f80u};
#pragma unroll
    for (int i = 0; i < 3; ++i) {
      const int R = wv + 4 * i;
      if (R < 11) {
        const int w = R / 3, i3 = R - 3 * w;
        f32x4 cs = {0.f, 0.f, 0.f, 0.f};
#pragma unroll
        for (int kk = 0; kk < 2; ++kk) {
          const char* src = smem + (32 * kk + 8 * g + tq) * ST + R * 32 + tp * 8;
          u32x2 lo = tr_read(src), hi = tr_read(src + 4 * ST);
          u32x4 o = {lo[0], lo[1], hi[0], hi[1]};
          st16_wt(ws + OFF_W13 + ((size_t)((ft * 4 + w) * 16 + kp * 2 + kk) * 3 + i3) * 1024 + lane * 16, o);
          cs = __builtin_amdgcn_mfma_f32_16x16x32_bf16(__builtin_bit_cast(bf16x8, o), __builtin_bit_cast(bf16x8, onesu), cs, 0, 0, 0);
        }
        if (r16 == 0) *(f32x4*)(ws + OFF_CS + ((size_t)(kp * 16 + ft) * 176 + R * 16 + 4 * g) * 4) = cs;
      }
    }
  } else if (b < PB_ST) {
    const int bb = b - PB_W2;
    const int ks = bb >> 1, hd = bb & 1;
    constexpr int ST = 512 + 32;
    u32x4 v[4];
#pragma unroll
    for (int it = 0; it < 4; ++it) {
      const int r = (tid >> 5) + 8 * it, c = (tid & 31) * 8;
      const int sl = ks * 32 + r;
      const int ft = sl / 88, s = sl - ft * 88;
      v[it] = (u32x4){0u, 0u, 0u, 0u};
      if (s < FT) v[it] = *(const u32x4*)(w2 + (size_t)(ft * FT + s) * D_ + hd * 256 + c);
    }
#pragma unroll
    for (int it = 0; it < 4; ++it) *(u32x4*)(smem + ((tid >> 5) + 8 * it) * ST + (tid & 31) * 16) = v[it];
    __syncthreads();
#pragma unroll
    for (int b2 = 0; b2 < 4; ++b2) {
      const char* src = smem + (8 * g + tq) * ST + (wv * 64 + b2 * 16) * 2 + tp * 8;
      u32x2 lo = tr_read(src), hi = tr_read(src + 4 * ST);
      st16_wt(ws + OFF_W2 + ((size_t)(hd * NKS2 + ks) * 16 + wv * 4 + b2) * 1024 + lane * 16,
              (u32x4){lo[0], lo[1], hi[0], hi[1]});
    }
  } else {
    const int row0 = (b - PB_ST) * 16 + wv * 4;
    u32x4 v[4];
#pragma unroll
    for (int r = 0; r < 4; ++r) v[r] = *(const u32x4*)(x0 + (size_t)(row0 + r) * D_ + lane * 8);
    if (wv == 0) {
      const int bb = b - PB_ST;
      if (lane < 16) st16_wt(ws + OFF_ST2 + (size_t)bb * 256 + lane * 16, (u32x4){0u, 0u, 0u, 0u});
      else if (lane < 48) st16_wt(ws + OFF_FL + (size_t)bb * 512 + (lane - 16) * 16, (u32x4){0u, 0u, 0u, 0u});
    }
    float sv = 0.f;
#pragma unroll
    for (int r = 0; r < 4; ++r) {
      float s1 = 0.f, s2 = 0.f;
#pragma unroll
      for (int j = 0; j < 4; ++j) {
        const float a = bflo(v[r][j]), c = bfhi(v[r][j]);
        s1 += a + c;
        s2 = fmaf(a, a, s2); s2 = fmaf(c, c, s2);
      }
      s1 = row16_sum(s1); s2 = row16_sum(s2);
      sv = r16 == 2 * r ? s1 : sv;
      sv = r16 == 2 * r + 1 ? s2 : sv;
    }
    sv += __shfl_xor(sv, 16);
    sv += __shfl_xor(sv, 32);
    if (lane < 8) *(double*)(ws + OFF_ST0 + (size_t)row0 * 16 + lane * 8) = (double)sv;
  }
  STAMP(5); STAMP_END();
}

constexpr int KC_CHUNK = 288;
constexpr int KC_COPY = 8 * KC_CHUNK + 64;
constexpr int KC_WAVE = 4 * KC_COPY;
constexpr int CV_UR = 4608;
constexpr int CL_UR = 4 * KC_WAVE, CL_RED = CL_UR + 2 * CV_UR, CL_END = CL_RED + 16384;
#define CV_PPRIO 0

__global__ __launch_bounds__(512) NOPK
void conv_kernel(const char* __restrict__ xT, const char* __restrict__ stats, const char* __restrict__ ws,
                 char* __restrict__ xoT DIAG_ARG) {
  STAMP_BEGIN();
  __shared__ __attribute__((aligned(16))) char smem[CL_END];
  const int tid = threadIdx.x, lane = tid & 63;
  const int wv = __builtin_amdgcn_readfirstlane(tid >> 6);
  const int d0 = chan_of_block(blockIdx.x);
  const int r16 = lane & 15, g = lane >> 4;
  if (wv < 4) {
    const int ch = wv >> 1, hw = wv & 1;
    char* kc = smem + wv * KC_WAVE;
    {
      const char* src = ws + OFF_KC + (size_t)(d0 + ch) * KC_ROW + 256 * hw;
      u32x4 a[3];
      u32x2 pr[3];
      int dsto[3];
#pragma unroll
      for (int h = 0; h < 3; ++h) {
        int pc = lane + 64 * h;
        pc = pc < 144 ? pc : 143;
        const int cp = pc / 18, pi = pc - 18 * cp;
        dsto[h] = cp * KC_CHUNK + pi * 16;
        const int so = cp * 512 + pi * 16;
        a[h] = *(const u32x4*)(src + so);
        pr[h] = *(const u32x2*)(src + (so + 256 * hw > 0 ? so - 8 : 0));
      }
      STAMP(13);
      if (lane < 16) *(u32x4*)(smem + CL_UR + ch * CV_UR + (256 + 16 * hw + lane) * 16) = (u32x4){0u, 0u, 0u, 0u};
#pragma unroll
      for (int h = 0; h < 3; ++h) {
        if (h == 2 && lane >= 16) break;
        u32 w0 = __builtin_amdgcn_alignbit(pr[h][1], pr[h][0], 16), w1 = __builtin_amdgcn_alignbit(a[h][0], pr[h][1], 16);
        u32 w2 = __builtin_amdgcn_alignbit(a[h][1], a[h][0], 16), w3 = __builtin_amdgcn_alignbit(a[h][2], a[h][1], 16);
        u32 w4 = __builtin_amdgcn_alignbit(a[h][3], a[h][2], 16);
        char* dst = kc + dsto[h];
        *(u32x4*)(dst) = a[h];
        *(u32x4*)(dst + KC_COPY) = (u32x4){w1, w2, w3, w4};
        *(u32x4*)(dst + 2 * KC_COPY) = (u32x4){pr[h][1], a[h][0], a[h][1], a[h][2]};
        *(u32x4*)(dst + 3 * KC_COPY) = (u32x4){w0, w1, w2, w3};
      }
    }
    wave_fence();
    const int aoff = wv * KC_WAVE + (3 - (r16 & 3)) * KC_COPY + 2 * (8 * g + 4 * ((r16 >> 2) + 1));
    int aoff2 = aoff + 8;
    tie(aoff2);
    const char* abase = smem + aoff;
    const char* abase2 = smem + aoff2;
    const char* bbase = smem + CL_UR + ch * CV_UR + 2 * (2032 - 16 * r16 + 8 * g) - 512 * 7 + 256 * hw;
    char* rbase = smem + CL_RED + wv * 2048 + lane * 16;
    f32x4 acc[8];
#pragma unroll
    for (int j = 0; j < 8; ++j) acc[j] = (f32x4){0.f, 0.f, 0.f, 0.f};
    f16x8 af[8][4], bf[2][2][4];
    auto ldA = [&](int cp, int wi) AI {
      u32x2 lo = *(const u32x2*)(abase + KC_CHUNK * cp + 64 * wi);
      u32x2 hi = *(const u32x2*)(abase2 + KC_CHUNK * cp + 64 * wi);
      af[cp][wi] = __builtin_bit_cast(f16x8, (u32x4){lo[0], lo[1], hi[0], hi[1]});
    };
#pragma unroll
    for (int cp = 0; cp < 2; ++cp)
#pragma unroll
      for (int wi = 0; wi < 4; ++wi) { ldA(cp, wi); SB0(); }
    auto ldB = [&](int s) AI {
#pragma unroll
      for (int wi = 0; wi < 4; ++wi)
#pragma unroll
        for (int y = 0; y < 2; ++y) bf[s & 1][y][wi] = *(const f16x8*)(bbase + 512 * (7 - 2 * s - y) + 64 * wi);
    };
    STAMP(2);
    raw_barrier();
    STAMP(3);
    ldB(0);
    static_for<0, 4>([&](auto S) AI {
      constexpr int s = S;
      static_for<0, 8 - 2 * s>([&](auto CP) AI {
        constexpr int cp = CP;
        if constexpr (s < 3 && cp == 6 - 2 * s) {
          SB0();
          STAMP(4 + 2 * s);
          raw_barrier();
          STAMP(5 + 2 * s);
          ldB(s + 1);
        }
#pragma unroll
        for (int wi = 0; wi < 4; ++wi) {
          SB0();
          acc[2 * s + cp] = __builtin_amdgcn_mfma_f32_16x16x32_f16(af[cp][wi], bf[s & 1][0][wi], acc[2 * s + cp], 0, 0, 0);
          if constexpr (2 * s + cp + 1 < 8)
            acc[2 * s + cp + 1] = __builtin_amdgcn_mfma_f32_16x16x32_f16(af[cp][wi], bf[s & 1][1][wi], acc[2 * s + cp + 1], 0, 0, 0);
          SB0();
          if constexpr (s == 0 && cp < 6) ldA(cp + 2, wi);
        }
        if constexpr (cp == (s >= 2 ? 1 : 2)) {
          *(f32x4*)(rbase + (s & 1) * 8192) = acc[2 * s];
          *(f32x4*)(rbase + (s & 1) * 8192 + 1024) = acc[2 * s + 1];
          SB0();
        }
      });
    });
    STAMP(10);
    raw_barrier();
  } else {
    __builtin_amdgcn_s_setprio(CV_PPRIO);
    const int pt = tid - 256, pw = wv - 4;
    f64x2 pv[4][2];
    u32 cx[4][2];
    auto issue = [&](int s) AI {
#pragma unroll
      for (int p = 0; p < 2; ++p) pv[s][p] = *(const f64x2*)(stats + s * 8192 + pt * 32 + p * 16);
#pragma unroll
      for (int c = 0; c < 2; ++c) cx[s][c] = *(const u32*)(xT + (size_t)(d0 + c) * (L_ * 2) + s * 1024 + pt * 4);
      SB0();
    };
    issue(0);
    issue(1);
    const float ki = ((const float*)(ws + OFF_KINV))[d0 + (pw & 1)];
    const size_t eoff = (size_t)(d0 + (pw & 1)) * (L_ * 2) + (512 * (pw >> 1) + 256 * (g & 1) + 16 * r16 + 4 * (g & 2)) * 2;
    u32x4 xr[2];
    SB0();
    auto norm = [&](int s) AI {
      f32x2 m, rs;
      ln_final2((f32x2){(float)pv[s][0][0], (float)pv[s][1][0]}, (f32x2){(float)pv[s][0][1], (float)pv[s][1][1]}, m, rs);
      rs = (f32x2){__builtin_amdgcn_rcpf(rs[0]), __builtin_amdgcn_rcpf(rs[1])};
#pragma unroll
      for (int c = 0; c < 2; ++c) {
        const f32x2 x = {bflo(cx[s][c]), bfhi(cx[s][c])};
        const f32x2 u = rbf2(rbf2(x - m) * rs);
        *(u32*)(smem + CL_UR + c * CV_UR + 4092 - 1024 * s - 4 * pt) = pk_f16(u[1], u[0]);
      }
      SB0();
    };
    auto epi = [&](int s) AI {
      if ((pw >> 1) != (s & 1)) return;
      u32 P[2][2];
#pragma unroll
      for (int y = 0; y < 2; ++y) {
        const char* rp = smem + CL_RED + (s & 1) * 8192 + (pw & 1) * 4096 + y * 1024 + lane * 16;
        const f32x4 a = (*(const f32x4*)rp + *(const f32x4*)(rp + 2048)) * ki;
        P[y][0] = pk_bf16(a[0], a[1]);
        P[y][1] = pk_bf16(a[2], a[3]);
      }
      swap16(P[0][0], P[1][0]);
      swap16(P[0][1], P[1][1]);
      u32x4 o;
#pragma unroll
      for (int i = 0; i < 4; ++i) {
        const u32 y = P[i >> 1][i & 1], x = xr[s >> 1][i];
        o[i] = pk_bf16(bflo(y) + bflo(x), bfhi(y) + bfhi(x));
      }
      st16_wt(xoT + eoff + (s >> 1) * 2048, o);
      SB0();
    };
    STAMP(2);
    norm(0);
    STAMP(3);
    raw_barrier();
    issue(2);
    issue(3);
    xr[0] = *(const u32x4*)(xT + eoff);
    xr[1] = *(const u32x4*)(xT + eoff + 2048);
    SB0();
    norm(1);
    norm(2);
    STAMP(4);
    raw_barrier();
    norm(3);
    epi(0);
    STAMP(5);
    raw_barrier();
    epi(1);
    raw_barrier();
    epi(2);
    raw_barrier();
    STAMP(6);
    epi(3);
  }
  STAMP(12); STAMP_END();
}

#define HFLAV "sc1"
template <int OFF>
DEVI void gld16(bf16x8& dst, u32 voff, const char* sbase) {
  asm volatile("global_load_dwordx4 %0, %1, %2 offset:%3" : "=v"(dst) : "v"(voff), "s"(sbase), "i"(OFF) : "memory");
}
DEVI void gld16c(bf16x8& dst, u32 voff, const char* sbase) {
  asm volatile("global_load_dwordx4 %0, %1, %2 " HFLAV : "=v"(dst) : "v"(voff), "s"(sbase) : "memory");
}
DEVI void gld8(u32x2& dst, u32 voff, const char* sbase) {
  asm volatile("global_load_dwordx2 %0, %1, %2" : "=v"(dst) : "v"(voff), "s"(sbase) : "memory");
}
constexpr int FF_CS = 131072;
constexpr int FF_CNT = FF_CS + 1008;
constexpr int FF_R1 = FF_CS + 1024;
constexpr int FF_R2 = FF_R1 + 512;
constexpr int FF_ST = FF_R2 + 2048;
constexpr int FF_DUMP = FF_ST + 8192;
constexpr int FF_OUT = FF_DUMP;
constexpr int FF_END = FF_DUMP + 16384;
DEVI constexpr int dump_off(int u) { return u < 8 ? (16 + u) * 4096 : FF_DUMP + (u - 8) * 4096; }
#define G1_RING 3
#define G2_PF 16
#define POLL_SLEEP 2
#define ST_ARG
#define ST_PASS

template <int NW>
DEVI void g1_epi(char* smem, const f32x4 (&acc)[NW][4], char* hbase, int w, int e, int lane, int ft) {
  const int r16 = lane & 15, g = lane >> 4;
  char* stp = smem + FF_ST + (2 * w + e) * 1024;
  {
    const float s1 = *(const float*)(smem + FF_R1 + (64 * e + lane) * 4);
    const float s2 = *(const float*)(smem + FF_R2 + (64 * e + lane) * 16 + (lane & 3) * 4);
    f32x2 m, sd;
    ln_final2((f32x2){s1, s1}, (f32x2){s2, s2}, m, sd);
    *(f32x4*)(stp + lane * 16) = (f32x4){-m[0], -1.44269504f * __builtin_amdgcn_rcpf(sd[0]), sd[0] * sd[0], 0.f};
  }
  f32x4 c4[NW];
#pragma unroll
  for (int i = 0; i < NW; ++i) c4[i] = *(const f32x4*)(smem + FF_CS + ((3 * w + i) * 16 + 4 * g) * 4);
  wave_fence();
  f32x4 st[4];
#pragma unroll
  for (int jj = 0; jj < 4; ++jj) st[jj] = *(const f32x4*)(stp + (16 * jj + r16) * 16);
  const u32 hv = g * 1024 + r16 * 16;
#pragma unroll
  for (int i = 0; i < NW; ++i) {
    u32 P[4];
#pragma unroll
    for (int jj = 0; jj < 4; ++jj) {
      float h[2];
#pragma unroll
      for (int z = 0; z < 2; ++z) {
        const float a = fmaf(st[jj][0], c4[i][z], acc[i][jj][z]);
        const float b = fmaf(st[jj][0], c4[i][2 + z], acc[i][jj][2 + z]);
        const float ex = __builtin_amdgcn_exp2f(a * st[jj][1]);
        h[z] = (a * b) * __builtin_amdgcn_rcpf(fmaf(ex, st[jj][2], st[jj][2]));
      }
      P[jj] = pk_bf16(h[0], h[1]);
    }
    swap32(P[0], P[2]); swap32(P[1], P[3]);
    swap16(P[0], P[1]); swap16(P[2], P[3]);
    const int Rg = 11 * ft + 3 * w + i;
    char* dst = hbase + (Rg >> 2) * 8192 + (Rg & 3) * 256 + e * 4096;
    st16_wt(dst + hv, (u32x4){P[0], P[1], P[2], P[3]});
    SB0();
  }
}
DEVI void publish(char* smem, char* fl, int lane, int ft) {
  u32 cnt = 0u;
  const u32 ca = FF_CNT + (u32)(uintptr_t)(LDS_AS char*)smem, one = 1u;
  if (lane == 0)
    asm volatile("ds_add_rtn_u32 %0, %1, %2\n\ts_waitcnt lgkmcnt(0)" : "=v"(cnt) : "v"(ca), "v"(one) : "memory");
  if (__builtin_amdgcn_readfirstlane(cnt) == 7u && lane < 16) {
    const u32 fo = lane * 128 + ft * 4;
    asm volatile("global_store_dword %0, %1, %2 sc1" ::"v"(fo), "v"(one), "s"(fl) : "memory");
  }
}

template <bool W3>
DEVI void g1_main(char* smem, const char* wbase, char* hbase, int wv, int lane, int ft, int c0 ST_ARG) {
  constexpr int NW = W3 ? 2 : 3;
  const int r16 = lane & 15, g = lane >> 4;
  const u32 lane16 = lane * 16;
  bf16x8 wf[16][NW];
  auto ld_w = [&](auto K, auto I) AI {
    constexpr int k = K, i = I;
    gld16<i * 1024>(wf[k][i], lane16, wbase + ((k + 2 * c0) & 15) * 3072);
  };
  auto ld_wk = [&](auto K) AI { static_for<0, NW>([&](auto I) AI { ld_w(K, I); }); };
  static_for<0, G1_RING>(ld_wk);
  const int q = r16 >> 2, p = lane & 3;
  const int hl = q | ((g & 1) << 2);
  int raddr[8];
#pragma unroll
  for (int j = 0; j < 8; ++j) raddr[j] = (8 * g + q) * 256 + 8 * p + ((j ^ hl) * 32);
  f32x4 acc[3][8];
  u32x2 ulo[2][8], uhi[2][8];
  auto rd_lo = [&](int buf, int ks, int lt) AI { ulo[buf][lt] = tr_read(smem + raddr[lt] + (32 * (ks & 7)) * 256); };
  auto rd_hi = [&](int buf, int ks, int lt) AI { uhi[buf][lt] = tr_read(smem + raddr[lt] + (32 * (ks & 7) + 4) * 256); };
  auto uf = [&](int buf, int lt) AI {
    return __builtin_bit_cast(bf16x8, (u32x4){ulo[buf][lt][0], ulo[buf][lt][1], uhi[buf][lt][0], uhi[buf][lt][1]});
  };
#pragma unroll
  for (int i = 0; i < 3; ++i)
#pragma unroll
    for (int j = 0; j < 8; ++j) acc[i][j] = (f32x4){0.f, 0.f, 0.f, 0.f};
  STAMP(2);
  bare_barrier();
  STAMP(3);
#pragma unroll
  for (int lt = 0; lt < 8; ++lt) { rd_lo(0, 0, lt); rd_hi(0, 0, lt); }
  static_for<0, 16>([&](auto KS) AI {
    constexpr int ks = KS;
    constexpr int cur = ks & 1, nxt = cur ^ 1;
    constexpr int hi = ks + G1_RING - 1 > 15 ? 15 : ks + G1_RING - 1;
    SB0();
    wait_vm<NW * (hi - ks)>();
#pragma unroll
    for (int i = 0; i < NW; ++i) tie(wf[ks][i]);
    SB0();
    static_for<0, 8>([&](auto LT) AI {
      constexpr int lt = LT;
      acc[0][lt] = __builtin_amdgcn_mfma_f32_16x16x32_bf16(wf[ks][0], uf(cur, lt), acc[0][lt], 0, 0, 0);
      SB0();
      if constexpr (lt == 1 && (ks & 1) && ks < 15) { bare_barrier(); SB0(); }
      if constexpr (ks == 7) { raddr[lt] += 65536; tie(raddr[lt]); }
      if constexpr (ks < 15 && lt >= 1) rd_lo(nxt, ks + 1, lt - 1);
      SB0();
      acc[1][lt] = __builtin_amdgcn_mfma_f32_16x16x32_bf16(wf[ks][1], uf(cur, lt), acc[1][lt], 0, 0, 0);
      SB0();
      if constexpr (ks < 15 && lt >= 1) rd_hi(nxt, ks + 1, lt - 1);
      SB0();
      acc[2][lt] = __builtin_amdgcn_mfma_f32_16x16x32_bf16(W3 ? uf(cur, lt) : wf[ks][NW - 1], uf(cur, lt), acc[2][lt], 0, 0, 0);
      SB0();
      if constexpr (lt >= 2 && lt < 2 + NW && ks + G1_RING <= 15)
        ld_w(std::integral_constant<int, (ks + G1_RING <= 15 ? ks + G1_RING : 15)>{}, std::integral_constant<int, lt - 2>{});
      if constexpr (ks < 15 && lt == 7) { rd_lo(nxt, ks + 1, 7); rd_hi(nxt, ks + 1, 7); }
      SB0();
    });
  });
  if constexpr (W3) {
    if (g == 3) {
#pragma unroll
      for (int j = 0; j < 8; ++j) *(float*)(smem + FF_R1 + (16 * j + r16) * 4) = acc[1][j][0];
    }
    if (g == q) {
#pragma unroll
      for (int j = 0; j < 8; ++j) *(f32x4*)(smem + FF_R2 + (16 * j + r16) * 16) = acc[2][j];
    }
  }
#pragma unroll
  for (int i = 0; i < NW; ++i)
#pragma unroll
    for (int jj = 0; jj < 4; ++jj) *(f32x4*)(smem + dump_off(4 * i + jj) + wv * 1024 + lane16) = acc[i][4 + jj];
  raw_barrier();
  STAMP(6);
  f32x4 a4[NW][4];
#pragma unroll
  for (int i = 0; i < NW; ++i)
#pragma unroll
    for (int jj = 0; jj < 4; ++jj) a4[i][jj] = acc[i][jj];
  g1_epi<NW>(smem, a4, hbase, wv, 0, lane, ft);
}

__global__ __launch_bounds__(512) NOPK
void ffn_kernel(const char* __restrict__ xT, char* __restrict__ ws, char* __restrict__ xoT, u16* __restrict__ out,
                int layer DIAG_ARG) {
  STAMP_BEGIN();
  __shared__ __attribute__((aligned(1024))) char smem[FF_END];
  const int tid = threadIdx.x, lane = tid & 63;
  const int wv = __builtin_amdgcn_readfirstlane(tid >> 6);
  const int hf = blockIdx.x & 1, lq = (blockIdx.x >> 1) & 3, idx = blockIdx.x >> 3;
  const int mt = 4 * lq + (idx & 3), j8 = idx >> 2, ft = 8 * hf + j8;
  const int lh = j8 & 1, nq = j8 >> 1, nt = 4 * hf + nq;
  const int l0 = 128 * mt + 64 * lh;
  const int r16 = lane & 15, g = lane >> 4;
  const u32 lane16 = lane * 16;
  char* fl = ws + OFF_FL + (layer * 16 + mt) * 2048;
  char* hbase = ws + OFF_H + (size_t)layer * HBYTES + (size_t)mt * (NKS2 * 8192);
  const int c0 = 0;
  const int rot = 0;
  if (wv < 4) {
    {
      const char* wbase = ws + OFF_W13 + (size_t)(ft * 4 + wv) * 49152;
      if (wv < 3) g1_main<false>(smem, wbase, hbase, wv, lane, ft, c0 ST_PASS);
      else g1_main<true>(smem, wbase, hbase, wv, lane, ft, c0 ST_PASS);
    }
    STAMP(7);
    u32x2 xres[4];
    {
      const u32 xv = r16 * (L_ * 2) + g * 8;
      const char* xb = xT + ((size_t)(64 * nt) * L_ + l0 + 16 * wv) * 2;
#pragma unroll
      for (int b2 = 0; b2 < 4; ++b2) gld8(xres[b2], xv, xb + (size_t)b2 * (16 * L_ * 2));
    }
    wait_vm<0>();
#pragma unroll
    for (int b2 = 0; b2 < 4; ++b2) tie(xres[b2]);
    STAMP(8);
    RSTAMP(14);
    publish(smem, fl, lane, ft);
    const char* hs[4];
#pragma unroll
    for (int sg = 0; sg < 4; ++sg) {
      hs[sg] = hbase + (4 * lh + wv) * 1024 + ((sg + rot) & 3) * (11 * 8192);
      ties(hs[sg]);
    }
    bare_barrier();
    STAMP(9);
    bf16x8 hfr[NKS2], wfr[3][4];
    auto ld_h = [&](auto J) AI {
      constexpr int j = J;
      gld16c(hfr[j], lane16, hs[j / 11] + (j % 11) * 8192);
    };
    wait_vm<0>();
    static_for<0, G2_PF>(ld_h);
    STAMP(10);
    const char* wl0 = smem + lane16;
    int hi16 = lane16 + 65536;
    tie(hi16);
    const char* wl1 = smem + hi16;
    auto rd_w = [&](int j, int b2) AI {
      const int sl = j & 31;
      wfr[j % 3][b2] = *(const bf16x8*)((sl < 16 ? wl0 : wl1) + (sl & 15) * 4096 + b2 * 1024);
    };
#pragma unroll
    for (int j = 0; j < 2; ++j)
#pragma unroll
      for (int b2 = 0; b2 < 4; ++b2) rd_w(j, b2);
    f32x4 acc[4];
#pragma unroll
    for (int b2 = 0; b2 < 4; ++b2) acc[b2] = (f32x4){0.f, 0.f, 0.f, 0.f};
    static_for<0, NKS2>([&](auto J) AI {
      constexpr int j = J;
      constexpr int hi = j + G2_PF - 1 > NKS2 - 1 ? NKS2 - 1 : j + G2_PF - 1;
      SB0();
      if constexpr (j == 12 || j == 24) bare_barrier();
      wait_vm<hi - j>();
      tie(hfr[j]);
      SB0();
#pragma unroll
      for (int b2 = 0; b2 < 4; ++b2) {
        acc[b2] = __builtin_amdgcn_mfma_f32_16x16x32_bf16(hfr[j], wfr[j % 3][b2], acc[b2], 0, 0, 0);
        SB0();
        if constexpr (j + 2 < NKS2) rd_w(j + 2, b2);
        if constexpr (j + G2_PF < NKS2) { if (b2 == 0) ld_h(std::integral_constant<int, (j + G2_PF < NKS2 ? j + G2_PF : 0)>{}); }
        SB0();
      }
    });
    STAMP(11);
    float xo[4][4];
#pragma unroll
    for (int b2 = 0; b2 < 4; ++b2) {
      u32x2 ov;
#pragma unroll
      for (int h = 0; h < 2; ++h) {
        const f32x2 y = rbf2((f32x2){acc[b2][2 * h], acc[b2][2 * h + 1]}) + (f32x2){bflo(xres[b2][h]), bfhi(xres[b2][h])};
        ov[h] = pk_bf16(y[0], y[1]);
        xo[b2][2 * h] = bflo(ov[h]); xo[b2][2 * h + 1] = bfhi(ov[h]);
      }
      *(u32x2*)(smem + FF_OUT + (16 * b2 + r16) * 144 + 32 * wv + 8 * g) = ov;
    }
    raw_barrier();
    STAMP(12);
    if (out == nullptr) {
      float sv = 0.f;
#pragma unroll
      for (int r = 0; r < 4; ++r) {
        const float s1 = row16_sum((xo[0][r] + xo[1][r]) + (xo[2][r] + xo[3][r]));
        const float s2 = row16_sum(fmaf(xo[0][r], xo[0][r], xo[1][r] * xo[1][r]) + fmaf(xo[2][r], xo[2][r], xo[3][r] * xo[3][r]));
        sv = r16 == 2 * r ? s1 : sv;
        sv = r16 == 2 * r + 1 ? s2 : sv;
      }
      if (r16 < 8)
        __builtin_amdgcn_global_atomic_fadd_f64(
            (__attribute__((address_space(1))) double*)(ws + OFF_ST2 + (size_t)(l0 + 16 * wv + 4 * g) * 16 + r16 * 8), (double)sv);
    }
  } else {
    const int lw = wv - 4;
    const u32 xv0 = g * (L_ * 2) + (((r16 >> 1) ^ g) * 32) + (r16 & 1) * 16;
    const u32 xv1 = xv0 ^ 128;
    const u32 lds0 = (u32)(uintptr_t)(LDS_AS char*)smem;
    u32 lds = lds0 + lw * 4096;
    const char* xb0 = xT + mt * 256 + (size_t)(16 * lw) * (L_ * 2);
    int cg = c0;
    auto chunk = [&]() AI {
      const char* xb = xb0 + (size_t)cg * (64 * L_ * 2);
#pragma unroll
      for (int n = 0; n < 4; ++n) glds16((n & 2) ? xv1 : xv0, lds + n * 1024, xb + (size_t)(4 * n) * (L_ * 2));
      lds += 16384;
      cg = (cg + 1) & 7;
    };
    chunk(); chunk(); chunk();
    if (tid == 256) *(u32*)(smem + FF_CNT) = 0u;
    const char* wsg[4];
#pragma unroll
    for (int sg = 0; sg < 4; ++sg) {
      wsg[sg] = ws + OFF_W2 + ((size_t)(hf * NKS2 + 11 * ((sg + rot) & 3)) * 16 + 4 * nq + lw) * 1024;
      ties(wsg[sg]);
    }
    const u32 ldw = lds0 + lw * 1024;
    auto wfill = [&](int j0) AI {
#pragma unroll
      for (int j = j0; j < j0 + 8; ++j) glds16(lane16, ldw + j * 4096, wsg[j / 11] + (size_t)(j % 11) * 16384);
    };
#pragma unroll 1
    for (int c = 0; c < 5; ++c) {
      wait_vm<8>();
      bare_barrier();
      chunk();
    }
    wait_vm<8>();
    bare_barrier();
    wfill(0);
    wait_vm<12>();
    bare_barrier();
    wfill(8);
    wait_vm<16>();
    bare_barrier();
    if (lw == 0) {
      u32x4 cp[8];
      const u32 cv = (lane < 44 ? lane : 0) * 16;
      const char* cb = ws + OFF_CS + ft * 704;
#pragma unroll
      for (int k = 0; k < 8; ++k)
        asm volatile("global_load_dwordx4 %0, %1, %2" : "=v"(cp[k]) : "v"(cv), "s"(cb + k * (16 * 704)) : "memory");
      wait_vm<0>();
#pragma unroll
      for (int k = 0; k < 8; ++k) tie(cp[k]);
      f32x4 c[8];
#pragma unroll
      for (int k = 0; k < 8; ++k) c[k] = __builtin_bit_cast(f32x4, cp[k]);
      const f32x4 s = ((c[0] + c[1]) + (c[2] + c[3])) + ((c[4] + c[5]) + (c[6] + c[7]));
      if (lane < 44) *(f32x4*)(smem + FF_CS + lane * 16) = s;
    }
    bf16x8 wr[12];
    static_for<0, 12>([&](auto T) AI {
      constexpr int j = 32 + T;
      gld16<0>(wr[j - 32], lane16, wsg[j / 11] + (size_t)(j % 11) * 16384);
    });
    raw_barrier();
    STAMP(6);
    wfill(24);
    if (lw < 3) {
      f32x4 a4[3][4];
#pragma unroll
      for (int u = 0; u < 12; ++u) a4[u >> 2][u & 3] = *(const f32x4*)(smem + dump_off(u) + lw * 1024 + lane16);
      asm volatile("s_waitcnt lgkmcnt(0)" ::: "memory");
#pragma unroll
      for (int u = 0; u < 12; ++u) tie(a4[u >> 2][u & 3]);
      wfill(16);
      g1_epi<3>(smem, a4, hbase, lw, 1, lane, ft);
    } else {
      f32x4 a4[2][4];
#pragma unroll
      for (int u = 0; u < 8; ++u) a4[u >> 2][u & 3] = *(const f32x4*)(smem + dump_off(u) + lw * 1024 + lane16);
      asm volatile("s_waitcnt lgkmcnt(0)" ::: "memory");
#pragma unroll
      for (int u = 0; u < 8; ++u) tie(a4[u >> 2][u & 3]);
      wfill(16);
      g1_epi<2>(smem, a4, hbase, lw, 1, lane, ft);
    }
    STAMP(7);
    wait_vm<0>();
#pragma unroll
    for (int t = 0; t < 12; ++t) tie(wr[t]);
    STAMP(8);
    RSTAMP(10);
    publish(smem, fl, lane, ft);
    if (lw == 3) {
      const u32 fo = ft * 128 + r16 * 4;
      u32 v0, v1, v2, v3, it;
#define POLL(a, b) "global_load_dword " a ", %5, %6 sc1\n\ts_waitcnt vmcnt(3)\n\tv_cmp_ne_u32 vcc, 1, " b "\n\ts_cbranch_vccz 2f\n\ts_sleep %7\n\t"
      asm volatile("s_waitcnt vmcnt(0)\n\ts_movk_i32 %4, 0x4000\n\t"
                   "global_load_dword %0, %5, %6 sc1\n\ts_sleep %7\n\t"
                   "global_load_dword %1, %5, %6 sc1\n\ts_sleep %7\n\t"
                   "global_load_dword %2, %5, %6 sc1\n\ts_sleep %7\n"
                   "1:\n\t" POLL("%3", "%0") POLL("%0", "%1") POLL("%1", "%2") POLL("%2", "%3")
                   "s_sub_u32 %4, %4, 1\n\ts_cmp_lg_u32 %4, 0\n\ts_cbranch_scc1 1b\n"
                   "2:\n\ts_barrier\n\ts_waitcnt vmcnt(0)"
                   : "=&v"(v0), "=&v"(v1), "=&v"(v2), "=&v"(v3), "=&s"(it)
                   : "v"(fo), "s"(fl), "i"(POLL_SLEEP) : "vcc", "scc", "memory");
    } else {
      bare_barrier();
    }
    STAMP(9);
    RSTAMP(11);
    bare_barrier();
#pragma unroll
    for (int t = 0; t < 12; ++t) *(bf16x8*)(smem + t * 4096 + lw * 1024 + lane16) = wr[t];
    raw_barrier();
    raw_barrier();
    STAMP(12);
  }
  if (out == nullptr) {
    const u32x4 v = *(const u32x4*)(smem + FF_OUT + (tid >> 3) * 144 + (tid & 7) * 16);
    st16_wt(xoT + ((size_t)(64 * nt + (tid >> 3)) * L_ + l0 + 8 * (tid & 7)) * 2, v);
  } else {
    const int lb = wv & 3, kk = wv >> 2;
    const char* src = smem + FF_OUT + (32 * kk + 8 * g + (r16 >> 2)) * 144 + lb * 32 + (lane & 3) * 8;
    const u32x2 lo = tr_read(src), hi = tr_read(src + 4 * 144);
    st16_wt(out + (size_t)(l0 + 16 * lb + r16) * D_ + 64 * nt + 32 * kk + 8 * g, (u32x4){lo[0], lo[1], hi[0], hi[1]});
  }
  STAMP(13); STAMP_END();
}

extern "C" void kernel_launch(void* const* d_in, const int* in_sizes, int n_in, void* d_out,
                              int out_size, void* d_ws, size_t ws_size, hipStream_t stream) {
  const u16* x0 = (const u16*)d_in[0];
  const u16* logA = (const u16*)d_in[1];
  const u16* Aim = (const u16*)d_in[2];
  const u16* Cre = (const u16*)d_in[3];
  const u16* Cim = (const u16*)d_in[4];
  const u16* logdt = (const u16*)d_in[5];
  const u16* Dsk = (const u16*)d_in[6];
  const u16* w1 = (const u16*)d_in[8];
  const u16* w3 = (const u16*)d_in[9];
  const u16* w2 = (const u16*)d_in[10];
  char* ws = (char*)d_ws;
  u16* out = (u16*)d_out;
  prep_kernel<<<PB_END, 256, 0, stream>>>(x0, logA, Aim, Cre, Cim, logdt, Dsk, w1, w3, w2, ws DIAG_PASS(0));
  conv_kernel<<<256, 512, 0, stream>>>(ws + OFF_X0, ws + OFF_ST0, ws, ws + OFF_X1 DIAG_PASS(1));
  ffn_kernel<<<256, 512, 0, stream>>>(ws + OFF_X1, ws, ws + OFF_X2, nullptr, 0 DIAG_PASS(2));
  conv_kernel<<<256, 512, 0, stream>>>(ws + OFF_X2, ws + OFF_ST2, ws, ws + OFF_X3 DIAG_PASS(3));
  ffn_kernel<<<256, 512, 0, stream>>>(ws + OFF_X3, ws, ws + OFF_X2, out, 1 DIAG_PASS(4));
}
```

```cpp
#include <hip/hip_runtime.h>
#include <hip/hip_bf16.h>
#include <stdint.h>
#include <type_traits>

typedef unsigned short u16;
typedef unsigned int u32;
typedef unsigned long long u64;
typedef float f32x4 __attribute__((ext_vector_type(4)));
typedef float f32x2 __attribute__((ext_vector_type(2)));
typedef __bf16 bf16x8 __attribute__((ext_vector_type(8)));
typedef __bf16 bf16x2 __attribute__((ext_vector_type(2)));
typedef _Float16 f16x8 __attribute__((ext_vector_type(8)));
typedef _Float16 f16x2 __attribute__((ext_vector_type(2)));
typedef short s16x4 __attribute__((ext_vector_type(4)));
typedef u32 u32x4 __attribute__((ext_vector_type(4)));
typedef u32 u32x3 __attribute__((ext_vector_type(3)));
typedef u32 u32x2 __attribute__((ext_vector_type(2)));
#define LDS_AS __attribute__((address_space(3)))
#define DEVI __device__ __forceinline__
#define SB0() __builtin_amdgcn_sched_barrier(0)
#define AI __attribute__((always_inline))

#define DIAG_LDS 0
#define STAMP_BEGIN(off)
#define STAMP(k)
#define STAMP_NEXT()
#define STAMP_END(k0, off)

template <int I, int N, class F>
DEVI void static_for(F&& f) {
  if constexpr (I < N) {
    f(std::integral_constant<int, I>{});
    static_for<I + 1, N>(f);
  }
}

constexpr int L_ = 2048, D_ = 512, N_ = 128, F_ = 1376;
constexpr int FT = 86;
constexpr int NKS2 = 44;
constexpr int KC_ROW = 4160;
constexpr size_t XBYTES = (size_t)L_ * D_ * 2;

constexpr size_t OFF_X0 = 0;
constexpr size_t OFF_FS = OFF_X0 + 4 * XBYTES;
constexpr size_t OFF_STP = OFF_FS + (size_t)2 * L_ * 8;
constexpr size_t OFF_KC = OFF_STP + (size_t)8 * L_ * 8;
constexpr size_t OFF_KINV = OFF_KC + (size_t)D_ * KC_ROW;
constexpr size_t OFF_W13 = OFF_KINV + 2048;
constexpr size_t OFF_CS = OFF_W13 + (size_t)16 * 196608;
constexpr size_t OFF_W2 = OFF_CS + (size_t)16 * 16 * 176 * 4;
constexpr size_t OFF_H = OFF_W2 + (size_t)NKS2 * 32 * 1024;
constexpr size_t HBYTES = (size_t)NKS2 * 128 * 1024;
constexpr size_t OFF_FR = (OFF_H + 2 * HBYTES + 32767) / 32768 * 32768;
constexpr size_t OFF_P0 = OFF_FR + (size_t)8 * L_ * 8;
constexpr size_t WS_END = OFF_P0 + (size_t)8 * 8 * 1024 * 8;

DEVI float u2f(u32 v) { return __builtin_bit_cast(float, v); }
DEVI u32 f2u(float v) { return __builtin_bit_cast(u32, v); }
DEVI float bf2f(u16 v) { return u2f(((u32)v) << 16); }
DEVI float bflo(u32 v) { return u2f(v << 16); }
DEVI float bfhi(u32 v) { return u2f(v & 0xffff0000u); }
DEVI u32 pk_bf16(float lo, float hi) {
  f32x2 v = {lo, hi};
  bf16x2 b = __builtin_convertvector(v, bf16x2);
  return __builtin_bit_cast(u32, b);
}
DEVI float rbf(float x) { return (float)((__bf16)x); }
DEVI u32 pk_f16(float lo, float hi) {
  f32x2 v = {lo, hi};
  f16x2 h = __builtin_convertvector(v, f16x2);
  return __builtin_bit_cast(u32, h);
}
template <int CTRL>
DEVI float dpp_add(float v) {
  return v + __builtin_bit_cast(float, __builtin_amdgcn_update_dpp(0, __builtin_bit_cast(int, v), CTRL, 0xf, 0xf, false));
}
DEVI float row16_sum(float v) {
  v = dpp_add<0x128>(v);
  v = dpp_add<0x124>(v);
  v = dpp_add<0x122>(v);
  v = dpp_add<0x121>(v);
  return v;
}
DEVI f32x2 rbf2(f32x2 v) {
  const u32 p = pk_bf16(v[0], v[1]);
  return (f32x2){bflo(p), bfhi(p)};
}
DEVI float op1(float x) { asm("" : "+v"(x)); return x; }
DEVI u32 pk_bf16s(float lo, float hi) { return pk_bf16(op1(lo), op1(hi)); }
DEVI f32x2 ln_final(float s1, float s2) {
  const float mean = op1(s1 * (1.0f / D_));
  const float var = fmaxf(fmaf(-mean, mean, op1(s2 * (1.0f / D_))), 0.f);
  const float sd = rbf(__builtin_amdgcn_sqrtf(rbf(rbf(var) + 1e-5f)));
  return (f32x2){rbf(mean), __builtin_amdgcn_rcpf(sd)};
}
DEVI float xor_add(float v, int m, int lane) {
  return v + __builtin_bit_cast(float, __builtin_amdgcn_ds_bpermute((lane ^ m) * 4, __builtin_bit_cast(int, v)));
}
DEVI void swap16(u32& x, u32& y) {
  auto r = __builtin_amdgcn_permlane16_swap(x, y, false, false);
  x = r[0]; y = r[1];
}
DEVI void swap32(u32& x, u32& y) {
  auto r = __builtin_amdgcn_permlane32_swap(x, y, false, false);
  x = r[0]; y = r[1];
}
DEVI void st16_wt(void* p, u32x4 v) {
  asm volatile("global_store_dwordx4 %0, %1, off sc1\n\ts_nop 1" ::"v"(p), "v"(v) : "memory");
}
DEVI void raw_barrier() {
  asm volatile("s_waitcnt lgkmcnt(0)" ::: "memory");
  __builtin_amdgcn_s_barrier();
  asm volatile("" ::: "memory");
}
DEVI void bare_barrier() {
  asm volatile("" ::: "memory");
  __builtin_amdgcn_s_barrier();
  asm volatile("" ::: "memory");
}
DEVI void glds16(u32 voff, u32 ldsaddr, const char* sbase) {
  asm volatile("s_mov_b32 m0, %1\n\ts_nop 0\n\tglobal_load_lds_dwordx4 %0, %2"
               ::"v"(voff), "s"(ldsaddr), "s"(sbase) : "memory");
}
DEVI void glds16s(u32 voff, u32 ldsaddr, const char* sbase) {
  asm volatile("s_mov_b32 m0, %1\n\ts_nop 0\n\tglobal_load_lds_dwordx4 %0, %2 sc1"
               ::"v"(voff), "s"(ldsaddr), "s"(sbase) : "memory");
}
template <int N>
DEVI void wait_vm() { asm volatile("s_waitcnt vmcnt(%0)" ::"i"(N) : "memory"); }
template <class T>
DEVI void tie(T& a) { asm volatile("" : "+v"(a)); }
template <class T>
DEVI void ties(T& a) { asm volatile("" : "+s"(a)); }
DEVI u32x2 tr_read(const char* p) {
  return __builtin_bit_cast(u32x2, __builtin_amdgcn_ds_read_tr16_b64_v4i16((LDS_AS s16x4*)p));
}
DEVI void wave_fence() {
  __builtin_amdgcn_fence(__ATOMIC_RELEASE, "wavefront");
  __builtin_amdgcn_wave_barrier();
  __builtin_amdgcn_fence(__ATOMIC_ACQUIRE, "wavefront");
}
#define PREP_SYM "_Z11prep_kernelPKtS0_S0_S0_S0_S0_S0_S0_S0_S0_Pc"
#define MAIN_SYM "_Z11main_kernelPcPt"
template <bool MAIN>
DEVI u32 code_fetch(int i, int lane) {
  u64 pc;
  int d;
  u32 last, v;
  if constexpr (MAIN)
    asm volatile("s_getpc_b64 %0\n1:\n\ts_mov_b32 %1, " MAIN_SYM "-1b\n\ts_mov_b32 %2, .Lmain_end-" MAIN_SYM : "=s"(pc), "=s"(d), "=s"(last));
  else
    asm volatile("s_getpc_b64 %0\n1:\n\ts_mov_b32 %1, " PREP_SYM "-1b\n\ts_mov_b32 %2, .Lprep_end-" PREP_SYM : "=s"(pc), "=s"(d), "=s"(last));
  const u32 off = i * 320 + (lane < 4 ? lane : 4) * 64;
  asm volatile("global_load_dword %0, %1, %2" : "=v"(v) : "v"(off < last ? off : last), "s"(pc + (long long)d) : "memory");
  return v;
}
DEVI int chan_of_block(int b) { return 256 * (b & 1) + 64 * ((b >> 1) & 3) + 2 * (b >> 3); }

DEVI f32x2 cmul(f32x2 a, f32x2 b) {
  const f32x2 t = (f32x2){a[0], a[0]} * b;
  return __builtin_elementwise_fma((f32x2){-a[1], a[1]}, (f32x2){b[1], b[0]}, t);
}

DEVI void row_stats(char* dst, int once, u32x4 a0, u32x4 a1, int lane) {
  const u32x4 onesu = {0x3f803f80u, 0x3f803f80u, 0x3f803f80u, 0x3f803f80u};
  const bf16x8 one = __builtin_bit_cast(bf16x8, onesu), x0 = __builtin_bit_cast(bf16x8, a0), x1 = __builtin_bit_cast(bf16x8, a1);
  f32x4 s1 = {0.f, 0.f, 0.f, 0.f}, s2 = s1;
  s1 = __builtin_amdgcn_mfma_f32_16x16x32_bf16(one, x0, s1, 0, 0, 0);
  s2 = __builtin_amdgcn_mfma_f32_16x16x32_bf16(x0, x0, s2, 0, 0, 0);
  s1 = __builtin_amdgcn_mfma_f32_16x16x32_bf16(one, x1, s1, 0, 0, 0);
  s2 = __builtin_amdgcn_mfma_f32_16x16x32_bf16(x1, x1, s2, 0, 0, 0);
  const float lo = (lane & 1) ? s2[1] : s2[0], hi = (lane & 1) ? s2[3] : s2[2];
  const f32x2 v = {s1[0], (lane & 2) ? hi : lo};
#define RSS(o) "global_store_dwordx2 %0, %1, %2 offset:" #o " sc1\n\t"
  asm volatile("s_mov_b64 exec, %3\n\t" RSS(-4096) "s_cmp_lg_u32 %4, 0\n\ts_cbranch_scc1 1f\n\t"
               RSS(-3072) RSS(-2048) RSS(-1024) RSS(0) RSS(1024) RSS(2048) RSS(3072) "\n1:\n\ts_mov_b64 exec, -1"
               ::"v"((lane & 15) * 8), "v"(v), "s"(dst + 4096), "s"(0xf0000f0000f0000full), "s"(once) : "scc", "memory");
}

constexpr int PB_W13 = 256, PB_ST = 512, PB_W2 = 640, PB_X = 728, PB_END = 984;
constexpr int KG_ROWB = 272;
constexpr int KG_BUF = 16 * KG_ROWB;
constexpr int KG_WAVE = 2 * KG_BUF;

DEVI void ld16(u32x4& dst, u32 voff, const void* sbase) {
  asm volatile("global_load_dwordx4 %0, %1, %2" : "=v"(dst) : "v"(voff), "s"(sbase) : "memory");
}
#define CF0M 28
#if defined(__HIP_DEVICE_COMPILE__)
#define PNOPK __attribute__((target("no-packed-fp32-ops")))
#else
#define PNOPK
#endif
__global__ __launch_bounds__(256, 4) PNOPK void prep_kernel(
    const u16* __restrict__ x0, const u16* __restrict__ logA, const u16* __restrict__ Aim,
    const u16* __restrict__ Cre, const u16* __restrict__ Cim, const u16* __restrict__ logdt,
    const u16* __restrict__ Dskip,
    const u16* __restrict__ w1, const u16* __restrict__ w3, const u16* __restrict__ w2,
    char* __restrict__ ws) {
  __shared__ __attribute__((aligned(16))) char smem[4 * KG_WAVE + 64 + DIAG_LDS / 2];
  STAMP_BEGIN(4 * KG_WAVE + 64);
  const int tid = threadIdx.x, lane = tid & 63, wv = tid >> 6;
  const int b = blockIdx.x;
  const int r16 = lane & 15, g = lane >> 4;
  const int tq = r16 >> 2, tp = lane & 3;
  u32 cf0 = code_fetch<false>(((b >> 1) & CF0M) + wv, lane);
  if (b < PB_W13) {
    const int ch = wv >> 1;
    const int dbase = chan_of_block(b);
    const int d = dbase + ch;
    const int n = 64 * (wv & 1) + lane;
    char* buf = smem + wv * KG_WAVE;
    float* red = (float*)(smem + 4 * KG_WAVE);
    const u16 i_dt = logdt[d], i_la = logA[d * N_ + n], i_ai = Aim[d * N_ + n], i_cr = Cre[d * N_ + n], i_ci = Cim[d * N_ + n];
    const u32 dsk2 = *(const u32*)(Dskip + dbase);
    const float dt = rbf(expf(bf2f(i_dt)));
    const float ea = rbf(expf(bf2f(i_la)));
    const float xl = (-ea * dt) * 1.44269504f;
    const float tr = (bf2f(i_ai) * dt) * 0.159154943f;
    f32x2 C = {2.0f * bf2f(i_cr) * dt, 2.0f * bf2f(i_ci) * dt};
    float isw;
    {
      float bs = row16_sum(fabsf(C[0]) + fabsf(C[1]));
      bs = xor_add(bs, 16, lane);
      bs = xor_add(bs, 32, lane);
      if (lane == 0) red[wv] = bs;
      int e = 0;
      if (bs > 0.f) {
        e = 14 - (((int)((f2u(bs) >> 23) & 0xff)) - 127);
        e = e > 100 ? 100 : (e < -100 ? -100 : e);
      }
      C *= u2f((u32)(127 + e) << 23);
      isw = u2f((u32)(127 - e) << 23);
    }
    auto cexp2k = [&](int k) AI {
      const float sc = (float)(1 << k);
      const float rk = __builtin_amdgcn_fractf(tr * sc);
      const float mag = __builtin_amdgcn_exp2f(xl * sc);
      return (f32x2){mag * __builtin_amdgcn_cosf(rk), mag * __builtin_amdgcn_sinf(rk)};
    };
    f32x2 Bk[12];
    Bk[0] = cexp2k(0); Bk[3] = cexp2k(3); Bk[7] = cexp2k(7); Bk[10] = cexp2k(10);
    Bk[3][1] = -Bk[3][1];
    Bk[1] = cmul(Bk[0], Bk[0]); Bk[4] = cmul(Bk[3], Bk[3]); Bk[8] = cmul(Bk[7], Bk[7]); Bk[11] = cmul(Bk[10], Bk[10]);
    Bk[2] = cmul(Bk[1], Bk[1]); Bk[5] = cmul(Bk[4], Bk[4]); Bk[9] = cmul(Bk[8], Bk[8]);
    Bk[6] = cmul(Bk[5], Bk[5]);
    STAMP(2);
    auto put = [&](int bf, int row, f32x2 v) AI {
      *(u32*)(buf + bf * KG_BUF + row * KG_ROWB + lane * 4) = pk_f16(v[0], v[1]);
    };
    auto frag = [&](int bf, int s) AI {
      return *(const f16x8*)(buf + bf * KG_BUF + r16 * KG_ROWB + (16 * s + 4 * g) * 4);
    };
    {
      f32x2 Q[16];
      Q[0] = (f32x2){1.f, 0.f};
      Q[1] = Bk[3]; Q[2] = Bk[4]; Q[3] = cmul(Q[1], Bk[4]);
      Q[4] = Bk[5]; Q[5] = cmul(Q[1], Bk[5]); Q[6] = cmul(Q[2], Bk[5]); Q[7] = cmul(Q[3], Bk[5]);
      Q[8] = Bk[6];
#pragma unroll
      for (int j = 1; j < 8; ++j) Q[8 + j] = cmul(Q[j], Bk[6]);
#pragma unroll
      for (int j = 0; j < 16; ++j) put(0, j, Q[j]);
    }
    f32x2 F[8];
    F[1] = Bk[0]; F[2] = Bk[1]; F[3] = cmul(Bk[0], Bk[1]);
    F[4] = Bk[2]; F[5] = cmul(F[1], Bk[2]); F[6] = cmul(F[2], Bk[2]); F[7] = cmul(F[3], Bk[2]);
    f32x2 CG[8];
    CG[0] = C;
    CG[1] = cmul(C, Bk[7]);
#pragma unroll
    for (int j1 = 0; j1 < 2; ++j1) {
      put(1, 8 * j1, CG[j1]);
#pragma unroll
      for (int bb = 1; bb < 8; ++bb) put(1, 8 * j1 + bb, cmul(CG[j1], F[bb]));
    }
    wave_fence();
    f16x8 bq[4];
    f32x4 tacc[3];
#pragma unroll
    for (int s = 0; s < 4; ++s) bq[s] = frag(0, s);
    tacc[0] = (f32x4){0.f, 0.f, 0.f, 0.f};
#pragma unroll
    for (int s = 0; s < 4; ++s) tacc[0] = __builtin_amdgcn_mfma_f32_16x16x32_f16(frag(1, s), bq[s], tacc[0], 0, 0, 0);
    wave_fence();
    CG[2] = cmul(C, Bk[8]); CG[3] = cmul(CG[1], Bk[8]);
#pragma unroll
    for (int j1 = 2; j1 < 4; ++j1) {
      put(0, 4 * (j1 - 2), CG[j1]);
#pragma unroll
      for (int bb = 1; bb < 4; ++bb) put(0, 4 * (j1 - 2) + bb, cmul(CG[j1], F[2 * bb]));
    }
#pragma unroll
    for (int j = 0; j < 4; ++j) {
      CG[4 + j] = cmul(CG[j], Bk[9]);
      put(0, 8 + 2 * j, CG[4 + j]);
      put(0, 9 + 2 * j, cmul(CG[4 + j], F[4]));
    }
#pragma unroll
    for (int j = 0; j < 8; ++j) put(1, j, cmul(CG[j], Bk[10]));
    put(1, 8, cmul(C, Bk[11]));
    wave_fence();
    tacc[1] = (f32x4){0.f, 0.f, 0.f, 0.f};
    tacc[2] = (f32x4){0.f, 0.f, 0.f, 0.f};
#pragma unroll
    for (int s = 0; s < 4; ++s) {
      tacc[1] = __builtin_amdgcn_mfma_f32_16x16x32_f16(frag(0, s), bq[s], tacc[1], 0, 0, 0);
      tacc[2] = __builtin_amdgcn_mfma_f32_16x16x32_f16(frag(1, s), bq[s], tacc[2], 0, 0, 0);
    }
    wave_fence();
    {
      float* tab = (float*)buf;
#pragma unroll
      for (int tile = 0; tile < 3; ++tile)
#pragma unroll
        for (int r = 0; r < 4; ++r) tab[(tile * 16 + 4 * g + r) * 16 + r16] = tacc[tile][r] * isw;
    }
    STAMP(3);
    raw_barrier();
    float scale[2];
#pragma unroll
    for (int c = 0; c < 2; ++c) {
      float mx = red[2 * c] + red[2 * c + 1] + fabsf(c ? bfhi(dsk2) : bflo(dsk2));
      int e = 0;
      if (mx > 0.f) {
        e = 14 - (((int)((f2u(mx) >> 23) & 0xff)) - 127);
        e = e > 100 ? 100 : (e < -100 ? -100 : e);
      }
      scale[c] = u2f((u32)(127 + e) << 23);
      if (tid == c) ((float*)(ws + OFF_KINV))[dbase + c] = u2f((u32)(127 - e) << 23);
    }
    auto tabv = [&](int c, int idx) AI {
      const float* t0 = (const float*)(smem + (2 * c) * KG_WAVE);
      const float* t1 = (const float*)(smem + (2 * c + 1) * KG_WAVE);
      return (t0[idx] + t1[idx]) * (c ? scale[1] : scale[0]);
    };
    auto first_idx = [&](int q) AI {
      int j1 = q >> 4, j0 = q & 15;
      int row = j1 < 2 ? 8 * j1 : (j1 < 4 ? 16 + 4 * (j1 - 2) : (j1 < 8 ? 24 + 2 * (j1 - 4) : 32 + (j1 - 8)));
      return row * 16 + j0;
    };
    auto kout = [&](int c, int q, u32x4 o) AI {
      *(u32x4*)(ws + OFF_KC + (size_t)(dbase + c) * KC_ROW + 32 + q * 16) = o;
    };
    if (wv == 0) {
      const int c = lane >> 5, q = lane & 31;
      const int i0 = first_idx(q);
      float v[8];
#pragma unroll
      for (int i = 0; i < 8; ++i) v[i] = tabv(c, i0 + 16 * i);
      if (q == 0) v[0] += c ? bfhi(dsk2) * scale[1] : bflo(dsk2) * scale[0];
      kout(c, q, (u32x4){pk_f16(v[0], v[1]), pk_f16(v[2], v[3]), pk_f16(v[4], v[5]), pk_f16(v[6], v[7])});
    } else if (wv == 1) {
      const int c = lane >> 5, q = 32 + (lane & 31);
      const int i0 = first_idx(q);
      float v0 = tabv(c, i0), v2 = tabv(c, i0 + 16), v4 = tabv(c, i0 + 32), v6 = tabv(c, i0 + 48);
      float v8 = tabv(c, first_idx(q + 1));
      kout(c, q, (u32x4){pk_f16(v0, v0), pk_f16(v2, v4), pk_f16(v4, v4), pk_f16(v6, v8)});
    } else {
      const int c = wv - 2, q = 64 + lane;
      const int i0 = first_idx(q);
      float v0 = tabv(c, i0), v4 = tabv(c, i0 + 16), v8 = tabv(c, first_idx(q + 1));
      kout(c, q, (u32x4){pk_f16(v0, v0), pk_f16(v0, v4), pk_f16(v4, v4), pk_f16(v8, v8)});
    }
    {
      const int c = wv >> 1, q = 128 + 64 * (wv & 1) + lane;
      float v0 = tabv(c, first_idx(q)), v8 = tabv(c, first_idx(q + 1));
      float vm = (q & 1) ? v8 : v0;
      kout(c, q, (u32x4){pk_f16(v0, v0), pk_f16(v0, v0), pk_f16(vm, v8), pk_f16(v8, v8)});
    }
    if (tid < 8) {
      const int c = tid >> 2, p = tid & 3;
      kout(c, p < 2 ? p - 2 : 254 + p, (u32x4){0u, 0u, 0u, 0u});
    }
  }
  int b2 = b;
  ties(b2);
  if (b2 < PB_W13) {
  } else if (b >= PB_X) {
    const int tb = b - PB_X;
    const int l0 = (tb >> 3) * 64, nt = tb & 7;
    constexpr int ST = 144;
    u32x4 v[2];
#pragma unroll
    for (int it = 0; it < 2; ++it) ld16(v[it], (32 * it + (tid >> 3)) * (D_ * 2) + (tid & 7) * 16, x0 + (size_t)l0 * D_ + nt * 64);
    u32 cf = code_fetch<true>((tb >> 3) * 4 + wv, lane);
    STAMP(2);
    wait_vm<1>();
    STAMP(3);
#pragma unroll
    for (int it = 0; it < 2; ++it) {
      tie(v[it]);
      *(u32x4*)(smem + (32 * it + (tid >> 3)) * ST + (tid & 7) * 16) = v[it];
    }
    raw_barrier();
#pragma unroll
    for (int kk = 0; kk < 2; ++kk) {
      const char* src = smem + (32 * (tp >> 1) + 8 * g + tq) * ST + wv * 32 + kk * 16 + (tp & 1) * 8;
      const u32x2 lo = tr_read(src), hi = tr_read(src + 4 * ST);
      v[kk] = (u32x4){lo[0], lo[1], hi[0], hi[1]};
    }
#pragma unroll
    for (int kk = 0; kk < 2; ++kk)
      st16_wt(ws + OFF_X0 + ((size_t)(nt * 64 + wv * 16 + 8 * kk + (r16 & 7)) * L_ + l0 + 32 * (r16 >> 3) + 8 * g) * 2, v[kk]);
#pragma unroll
    for (int it = 0; it < 2; ++it) st16_wt(ws + OFF_X0 + XBYTES + (size_t)tb * 8192 + it * 4096 + tid * 16, (u32x4){~0u, ~0u, ~0u, ~0u});
    STAMP(4);
    wait_vm<4>();
    tie(cf);
  } else if (b < PB_ST) {
    const int bb = b - PB_W13;
    const int ft = bb >> 4, ks = bb & 15;
    constexpr int ST = 368;
    u32x4 va[2], vb[2];
    int r[2], gr[2];
#pragma unroll
    for (int it = 0; it < 2; ++it) {
      const int idx = it == 0 || tid < 96 ? tid + it * 256 : 351;
      r[it] = (idx * 745) >> 13;
      gr[it] = idx - r[it] * 11;
      const u32 o = r[it] * (F_ * 2) + gr[it] * 16 - (gr[it] == 10 ? 4 : 0);
      ld16(va[it], o, w1 + (size_t)ks * 32 * F_ + ft * FT);
      ld16(vb[it], o, w3 + (size_t)ks * 32 * F_ + ft * FT);
    }
    STAMP(2);
    static_for<0, 2>([&](auto I) AI {
      constexpr int it = I;
      wait_vm<2 - 2 * it>();
      if constexpr (it == 1) STAMP(3);
      tie(va[it]); tie(vb[it]);
      const bool e = gr[it] == 10;
      const u32x4 a = va[it], c = vb[it];
      char* dst = smem + r[it] * ST + gr[it] * 32;
      *(u32x4*)dst = (u32x4){e ? a[1] : a[0], e ? c[1] : c[0], e ? a[2] : a[1], e ? c[2] : c[1]};
      *(u32x4*)(dst + 16) = (u32x4){e ? a[3] : a[2], e ? c[3] : c[2], e ? 0x00003f80u : a[3], e ? 0u : c[3]};
    });
    raw_barrier();
    const u32x4 onesu = {0x3f803f80u, 0x3f803f80u, 0x3f803f80u, 0x3f803f80u};
    const int wu = __builtin_amdgcn_readfirstlane(wv);
    u32x4 o[3];
#pragma unroll
    for (int i = 0; i < 3; ++i) {
      const char* src = smem + (8 * g + tq) * ST + (wu + 4 * i) * 32 + tp * 8;
      const u32x2 lo = tr_read(src), hi = tr_read(src + 4 * ST);
      o[i] = (u32x4){lo[0], lo[1], hi[0], hi[1]};
    }
#pragma unroll
    for (int i = 0; i < 3; ++i) {
      const int R = wu + 4 * i;
      if (i < 2 || wu < 3) {
        const int w = R / 3, i3 = R - 3 * w;
        st16_wt(ws + OFF_W13 + ((size_t)((ft * 4 + w) * 16 + ks) * 3 + i3) * 1024 + lane * 16, o[i]);
        const f32x4 cs = __builtin_amdgcn_mfma_f32_16x16x32_bf16(__builtin_bit_cast(bf16x8, o[i]), __builtin_bit_cast(bf16x8, onesu),
                                                                 (f32x4){0.f, 0.f, 0.f, 0.f}, 0, 0, 0);
        if (r16 == 0) *(f32x4*)(ws + OFF_CS + ((size_t)(ks * 16 + ft) * 176 + R * 16 + 4 * g) * 4) = cs;
      }
    }
  } else if (b >= PB_W2) {
    const int bb = b - PB_W2;
    const int ks = bb >> 1, hd = bb & 1;
    constexpr int ST = 512 + 32;
    u32x4 v[4];
    bool pad[4];
#pragma unroll
    for (int it = 0; it < 4; ++it) {
      const int kh = (ks + 10) >> 5, j = ks - 22 * kh, t = (4 * j + it) & 31, t3 = (t * 11) >> 5, m = -(j >> 4);
      const int ft = 8 * kh + ((j & 7 & ~m) | (t3 & m));
      const int s = 8 * (((3 * it + (j >> 3)) & ~m) | ((3 * (t - 3 * t3) + 2) & m)) + (tid >> 5);
      pad[it] = s >= FT;
      ld16(v[it], (s < FT ? s : 0) * (D_ * 2) + (tid & 31) * 16, w2 + (size_t)ft * FT * D_ + hd * 256);
    }
    STAMP(2);
    wait_vm<0>();
    STAMP(3);
#pragma unroll
    for (int it = 0; it < 4; ++it) {
      tie(v[it]);
      *(u32x4*)(smem + ((tid >> 5) + 8 * it) * ST + (tid & 31) * 16) = pad[it] ? (u32x4){0u, 0u, 0u, 0u} : v[it];
    }
    raw_barrier();
#pragma unroll
    for (int b2 = 0; b2 < 4; ++b2) {
      const char* src = smem + (8 * g + tq) * ST + (wv * 64 + b2 * 16) * 2 + tp * 8;
      const u32x2 lo = tr_read(src), hi = tr_read(src + 4 * ST);
      v[b2] = (u32x4){lo[0], lo[1], hi[0], hi[1]};
    }
#pragma unroll
    for (int b2 = 0; b2 < 4; ++b2) st16_wt(ws + OFF_W2 + ((size_t)(hd * NKS2 + ks) * 16 + wv * 4 + b2) * 1024 + lane * 16, v[b2]);
  } else {
    const int row0 = (b - PB_ST) * 16 + wv * 4;
    u32x4 v[4];
#pragma unroll
    for (int r = 0; r < 4; ++r) ld16(v[r], (wv * 4 + r) * (D_ * 2) + lane * 16, x0 + (size_t)(b - PB_ST) * 16 * D_);
    wait_vm<0>();
    float t1 = 0.f, t2 = 0.f;
#pragma unroll
    for (int r = 0; r < 4; ++r) {
      tie(v[r]);
      float s1 = 0.f, s2 = 0.f;
#pragma unroll
      for (int j = 0; j < 4; ++j) {
        const float a = bflo(v[r][j]), c = bfhi(v[r][j]);
        s1 += a + c;
        s2 = fmaf(a, a, s2); s2 = fmaf(c, c, s2);
      }
      s1 = row16_sum(s1); s2 = row16_sum(s2);
      t1 = r16 == r ? s1 : t1;
      t2 = r16 == r ? s2 : t2;
    }
    t1 = xor_add(t1, 16, lane); t2 = xor_add(t2, 16, lane);
    t1 = xor_add(t1, 32, lane); t2 = xor_add(t2, 32, lane);
    if (lane < 4) *(f32x2*)(ws + OFF_FS + (size_t)(row0 + lane) * 8) = ln_final(t1, t2);
  }
  STAMP(5); STAMP_END(0, 4 * KG_WAVE + 64);
  tie(cf0);
  asm volatile(".Lprep_end:");
}

constexpr int KC_CHUNK = 288;
constexpr int KC_COPY = 8 * KC_CHUNK + 64;
constexpr int KC_WAVE = 4 * KC_COPY;
constexpr int CV_UR = 4608;
constexpr int CL_KC = 65536, CL_XR = 104448, CL_RED = 114688, CL_UR = 148992, CL_END = CL_UR + 2 * CV_UR;
#define ST_ARG
#define ST_PASS
#define WPF_S 1
#define EARLY_LQ 0
#define XSL 8
#define FCOPY (bid & 7)
#define RETRY_SLEEP 2
template <int OFF, class T>
DEVI void gld16s(T& dst, u32 voff, const char* sbase) {
  asm volatile("global_load_dwordx4 %0, %1, %2 offset:%3 sc1" : "=v"(dst) : "v"(voff), "s"(sbase), "i"(OFF) : "memory");
}
template <int OFF>
DEVI void gld4s(u32& dst, u32 voff, const char* sbase) {
  asm volatile("global_load_dword %0, %1, %2 offset:%3 sc1" : "=v"(dst) : "v"(voff), "s"(sbase), "i"(OFF) : "memory");
}
DEVI u32x4 all_ones() {
  u32 m;
  asm volatile("v_mov_b32 %0, -1" : "=v"(m));
  return (u32x4){m, m, m, m};
}
DEVI u32 umax(u32 a, u32 b) { return a > b ? a : b; }
DEVI bool again(unsigned long long missing, u32& budget) {
  u32 r;
  asm volatile("s_cmp_lg_u64 %2, 0\n\ts_cselect_b32 %0, %1, 0\n\ts_min_u32 %0, %0, 1\n\ts_sub_u32 %1, %1, %0" : "=&s"(r), "+s"(budget) : "s"(missing) : "scc");
  return __builtin_expect(r != 0u, 0);
}
DEVI void nap(int n) {
  asm volatile("s_cmp_eq_u32 %0, 0\n\ts_cbranch_scc1 2f\n1:\n\ts_sleep 1\n\ts_sub_u32 %0, %0, 1\n\ts_cmp_lg_u32 %0, 0\n\ts_cbranch_scc1 1b\n2:" : "+s"(n) :: "scc", "memory");
}

struct KRegs { u32x4 a[3]; u32x2 pr[3]; };
DEVI void k_load(KRegs& k, const char* ws, int d0, int w, int lane) {
  const char* src = ws + OFF_KC + (size_t)(d0 + (w >> 1)) * KC_ROW + 256 * (w & 1);
#pragma unroll
  for (int h = 0; h < 3; ++h) {
    int pc = lane + 64 * h;
    pc = pc < 144 ? pc : 143;
    const int cp = (int)(__umul24(pc, 57) >> 10), pi = pc - 18 * cp;
    const int so = cp * 512 + pi * 16;
    asm volatile("global_load_dwordx4 %0, %1, %2" : "=v"(k.a[h]) : "v"(so), "s"(src) : "memory");
    asm volatile("global_load_dwordx2 %0, %1, %2 offset:-8" : "=v"(k.pr[h]) : "v"(so + 256 * (w & 1) > 0 ? so : 8), "s"(src) : "memory");
  }
}
DEVI void k_store(KRegs& k, char* smem, int w, int lane) {
#pragma unroll
  for (int h = 0; h < 3; ++h) { tie(k.a[h]); tie(k.pr[h]); }
#pragma unroll
  for (int h = 0; h < 3; ++h) {
    const int pc = lane + 64 * h;
    const int cp = (int)(__umul24(pc, 57) >> 10), pi = pc - 18 * cp;
    const u32x4 a = k.a[h];
    const u32x2 pr = k.pr[h];
    u32 w0 = __builtin_amdgcn_alignbit(pr[1], pr[0], 16), w1 = __builtin_amdgcn_alignbit(a[0], pr[1], 16);
    u32 w2 = __builtin_amdgcn_alignbit(a[1], a[0], 16), w3 = __builtin_amdgcn_alignbit(a[2], a[1], 16);
    u32 w4 = __builtin_amdgcn_alignbit(a[3], a[2], 16);
    int da = CL_KC + cp * KC_CHUNK + pi * 16;
    tie(da);
    char* dst = smem + w * KC_WAVE + (h == 2 && lane >= 16 ? lane * 16 : da);
    *(u32x4*)(dst) = a;
    *(u32x4*)(dst + KC_COPY) = (u32x4){w1, w2, w3, w4};
    u32 a1 = a[1];
    tie(a1);
    *(u32x4*)(dst + 2 * KC_COPY) = (u32x4){pr[1], a[0], a1, a[2]};
    *(u32x4*)(dst + 3 * KC_COPY) = (u32x4){w0, w1, w2, w3};
  }
}

struct XT { const char* xb; u32 lds, xv, pa, pb; };
DEVI XT x_tile(const char* smem, const char* xT, int mt, int lw, int lane) {
  XT x;
  x.xb = xT + mt * 256 + (size_t)(16 * lw) * (L_ * 2);
  x.lds = (u32)(uintptr_t)(LDS_AS char*)smem + lw * 4096;
  x.xv = (lane >> 4) * (L_ * 2) + ((((lane & 15) >> 1) ^ (lane >> 4)) * 32) + (lane & 1) * 16;
  u32 ra = 64 + lane, rb = (lane >> 3) * 64 + 2 * (lane & 7) + 1;
  tie(ra); tie(rb);
  x.pa = (lane & 15) * (L_ * 2);
  x.pb = (lane < 16 ? ra : rb) * (L_ * 2);
  return x;
}
template <int Q>
DEVI void x_step(const XT& x, u32 (&pr)[2], int skip) {
#define XSKIP "s_cmp_lg_u32 %3, 0\n\ts_cbranch_scc1 1f\n\t"
  if constexpr (Q == 0 || Q == 5) {
    asm volatile(XSKIP "global_load_dword %0, %1, %2 offset:252 sc1\n1:"
                 : "=v"(pr[Q / 5]) : "v"(Q ? x.pb : x.pa), "s"(x.xb), "s"(skip) : "scc", "memory");
  } else {
    constexpr int t = Q < 5 ? Q - 1 : Q - 2, c = t >> 2, n = t & 3;
    asm volatile(XSKIP "s_mov_b32 m0, %1\n\ts_nop 0\n\tglobal_load_lds_dwordx4 %0, %2 sc1\n1:"
                 ::"v"((n & 2) ? x.xv ^ 128 : x.xv), "s"(x.lds + c * 16384 + n * 1024), "s"(x.xb + (size_t)(64 * c + 4 * n) * (L_ * 2)),
                 "s"(skip) : "scc", "memory");
  }
}

DEVI void conv_m(char* smem, KRegs& k, bf16x8 (&w0)[3][3], u32 (&pr)[2], const char* wbase, const char* hp, const char* hq,
                 const char* xp, const char* sp, const char* xT, int mt, int late, int layer, int wv, int lane ST_ARG) {
  STAMP(13);
  wait_vm<0>();
  STAMP(14);
  u32x4 ones = all_ones();
  asm volatile("global_store_dwordx4 %0, %1, %2 sc1\n\tglobal_store_dwordx4 %0, %1, %2 offset:1024 sc1\n\t"
               "s_cmp_lg_u32 %6, 0\n\ts_cbranch_scc1 1f\n\t"
               "global_store_dwordx4 %0, %1, %3 sc1\n\tglobal_store_dwordx4 %0, %1, %3 offset:1024 sc1\n\t"
               "global_store_dwordx4 %4, %1, %5 sc1\n1:\n\ts_nop 1"
               ::"v"(lane * 16), "v"(ones), "s"(hp), "s"(xp), "v"((lane & 7) * 16 + (lane >= 8 ? (int)(OFF_FR - OFF_STP) - 131072 + (lane < 40 ? lane & 56 : 40) * 16384 : 0)), "s"(sp), "s"(layer) : "scc", "memory");
  k_store(k, smem, wv, lane);
  wave_fence();
  const int r16 = lane & 15, g = lane >> 4;
  const int ch = wv >> 1, hw = wv & 1;
  const int aoff = CL_KC + wv * KC_WAVE + (3 - (r16 & 3)) * KC_COPY + 2 * (8 * g + 4 * ((r16 >> 2) + 1));
  int aoff2 = aoff + 8;
  tie(aoff2);
  const char* abase = smem + aoff;
  const char* abase2 = smem + aoff2;
  const char* bbase = smem + CL_UR + ch * CV_UR + 2 * (2032 - 16 * r16 + 8 * g) - 512 * 7 + 256 * hw;
  char* rbase = smem + CL_RED + wv * 2048 + lane * 16;
  f32x4 acc[8];
#pragma unroll
  for (int j = 0; j < 8; ++j) acc[j] = (f32x4){0.f, 0.f, 0.f, 0.f};
  f16x8 af[8][4], bf[2][2][4];
  auto ldA = [&](int cp, int wi) AI {
    u32x2 lo = *(const u32x2*)(abase + KC_CHUNK * cp + 64 * wi);
    u32x2 hi = *(const u32x2*)(abase2 + KC_CHUNK * cp + 64 * wi);
    af[cp][wi] = __builtin_bit_cast(f16x8, (u32x4){lo[0], lo[1], hi[0], hi[1]});
  };
#pragma unroll
  for (int cp = 0; cp < 2; ++cp)
#pragma unroll
    for (int wi = 0; wi < 4; ++wi) { ldA(cp, wi); SB0(); }
  auto ldB = [&](int s) AI {
#pragma unroll
    for (int wi = 0; wi < 4; ++wi)
#pragma unroll
      for (int y = 0; y < 2; ++y) bf[s & 1][y][wi] = *(const f16x8*)(bbase + 512 * (7 - 2 * s - y) + 64 * wi);
  };
  XT xt;
  if constexpr (EARLY_LQ == 0) asm volatile("" : "=v"(pr[0]), "=v"(pr[1]));
  STAMP(2);
  raw_barrier();
  STAMP(3);
  ldB(0);
  static_for<0, 4>([&](auto S) AI {
    constexpr int s = S;
    static_for<0, 8 - 2 * s>([&](auto CP) AI {
      constexpr int cp = CP;
      if constexpr (s < 3 && cp == 6 - 2 * s) {
        SB0();
        if constexpr (s == 1) wait_vm<(WPF_S == 1 ? 9 : 0)>();
        STAMP(4 + 2 * s);
        raw_barrier();
        STAMP(5 + 2 * s);
        ldB(s + 1);
      }
#pragma unroll
      for (int wi = 0; wi < 4; ++wi) {
        SB0();
        acc[2 * s + cp] = __builtin_amdgcn_mfma_f32_16x16x32_f16(af[cp][wi], bf[s & 1][0][wi], acc[2 * s + cp], 0, 0, 0);
        if constexpr (2 * s + cp + 1 < 8)
          acc[2 * s + cp + 1] = __builtin_amdgcn_mfma_f32_16x16x32_f16(af[cp][wi], bf[s & 1][1][wi], acc[2 * s + cp + 1], 0, 0, 0);
        SB0();
        if constexpr (s == 0 && cp < 6) ldA(cp + 2, wi);
        if constexpr (s == 0 && cp < 4)
          if (wi == 1)
            asm volatile("global_store_dwordx4 %0, %1, %2 sc1" ::"v"(lane * 16), "v"(ones), "s"(hq + cp * 4096 - (cp == 3 ? ch * 2048 : 0)) : "memory");
        if constexpr (s >= 2 && EARLY_LQ > 0) {
          constexpr int q0 = 16 * (s - 2) + 4 * cp - XSL;
          if (wi == 0) { if constexpr (q0 < 0 && q0 >= -4) xt = x_tile(smem, xT, mt, wv, lane); }
          if (wi == 0) { if constexpr (q0 >= 0 && q0 < 14) x_step<(q0 >= 0 ? q0 : 0)>(xt, pr, late); }
          if (wi == 1) { if constexpr (q0 + 1 >= 0 && q0 + 1 < 14) x_step<(q0 + 1 >= 0 ? q0 + 1 : 0)>(xt, pr, late); }
          if (wi == 2) { if constexpr (q0 + 2 >= 0 && q0 + 2 < 14) x_step<(q0 + 2 >= 0 ? q0 + 2 : 0)>(xt, pr, late); }
          if (wi == 3) { if constexpr (q0 + 3 >= 0 && q0 + 3 < 14) x_step<(q0 + 3 >= 0 ? q0 + 3 : 0)>(xt, pr, late); }
        }
        if constexpr (s == WPF_S && cp < 3)
          if (wi < 3) {
            const int q = 3 * cp + wi, kk = q < 3 ? q : (q - 3) >> 1, ii = q < 3 ? 2 : (q - 3) & 1;
            asm volatile("global_load_dwordx4 %0, %1, %2" : "=v"(w0[kk][ii]) : "v"(lane * 16), "s"(wbase + kk * 3072 + ii * 1024) : "memory");
          }
      }
      if constexpr (s == 0 && cp == 3) tie(ones);
      if constexpr (cp == (s >= 2 ? 1 : 2)) {
        *(f32x4*)(rbase + (s & 1) * 8192) = acc[2 * s];
        *(f32x4*)(rbase + (s & 1) * 8192 + 1024) = acc[2 * s + 1];
        SB0();
      }
    });
  });
  STAMP(10);
  raw_barrier();
}

template <class F>
DEVI void conv_h(char* smem, const char* xin, const char* fsp, int fst, const char* f0p, int layer, int red, char* xo, const char* kip, u32& budget, int pw, int lane, F&& mid ST_ARG) {
  const int r16 = lane & 15, g = lane >> 4;
  const int pt = 64 * pw + lane;
  u32x4 fs[4];
  u32 cx[4][2];
  u32x4 ps[2][7];
#pragma unroll
  for (int j = 0; j < 14; ++j) ps[j / 7][j % 7] = (u32x4){0u, 0u, 0u, 0u};
  asm volatile("" : "=v"(fs[0]), "=v"(fs[1]), "=v"(cx[0][0]), "=v"(cx[0][1]), "=v"(cx[1][0]), "=v"(cx[1][1]));
  auto issue = [&](auto S, int skip = 0) AI {
    constexpr int s = S;
    if constexpr (s < 2) {
#define PL(i, o) "global_load_dwordx4 %" #i ", %10, %11 offset:" #o " sc1\n\t"
      asm volatile("s_cmp_lg_u32 %13, 0\n\ts_cbranch_scc1 2f\n\t" PL(0, 0) "s_cmp_eq_u32 %12, 0\n\ts_cbranch_scc1 1f\n\t"
                   PL(1, 128) PL(2, 256) PL(3, 384) PL(4, 512) PL(5, 640) PL(6, 768) PL(7, 896) "\n1:\n\t"
                   "global_load_dword %8, %14, %15 offset:%17 sc1\n\tglobal_load_dword %9, %14, %16 offset:%17 sc1\n2:"
                   : "+v"(fs[s]), "+v"(ps[s][0]), "+v"(ps[s][1]), "+v"(ps[s][2]), "+v"(ps[s][3]), "+v"(ps[s][4]), "+v"(ps[s][5]),
                     "+v"(ps[s][6]), "+v"(cx[s][0]), "+v"(cx[s][1])
                   : "v"(((pt >> 3) << (7 + 6 * layer)) + (pt & 7) * 16), "s"(f0p + ((s * 4096) << (6 * layer))), "s"(layer), "s"(skip),
                     "v"(pt * 4), "s"(xin), "s"(xin + L_ * 2), "i"(s * 1024) : "scc", "memory");
    } else {
      gld16s<0>(fs[s], pt * 16, fsp + s * fst);
      gld4s<s * 1024>(cx[s][0], pt * 4, xin);
      gld4s<s * 1024>(cx[s][1], pt * 4, xin + L_ * 2);
    }
    SB0();
  };
  auto norm = [&](auto S) AI {
    constexpr int s = S;
    auto chk = [&]() AI {
      tie(fs[s]); tie(cx[s][0]); tie(cx[s][1]);
      u32 t = umax(umax(umax(fs[s][0], fs[s][1]), fs[s][2]), umax(umax(fs[s][3], cx[s][0]), cx[s][1]));
      if constexpr (s < 2) {
#pragma unroll
        for (int j = 0; j < 7; ++j) {
          tie(ps[s][j]);
          t = umax(umax(t, ps[s][j][0]), umax(umax(ps[s][j][1], ps[s][j][2]), ps[s][j][3]));
        }
      }
      return __builtin_amdgcn_ballot_w64(t == ~0u);
    };
    if (again(chk(), budget)) do {
      __builtin_amdgcn_s_sleep(RETRY_SLEEP);
      issue(S);
      wait_vm<0>();
    } while (again(chk(), budget));
    if constexpr (s == 0) issue(std::integral_constant<int, 1>(), 1 - layer);
    if constexpr (s < 2)
      if (layer) {
        float t[4];
#pragma unroll
        for (int i = 0; i < 4; ++i) {
          t[i] = u2f(fs[s][i]);
#pragma unroll
          for (int j = 0; j < 7; ++j) t[i] = op1(t[i] + u2f(ps[s][j][i]));
        }
        const f32x2 fa = ln_final(t[0], t[1]), fb = ln_final(t[2], t[3]);
        fs[s] = (u32x4){f2u(fa[0]), f2u(fa[1]), f2u(fb[0]), f2u(fb[1])};
      }
#pragma unroll
    for (int c = 0; c < 2; ++c) {
      u32 u = pk_bf16s(bflo(cx[s][c]) - u2f(fs[s][0]), bfhi(cx[s][c]) - u2f(fs[s][2]));
      u = pk_bf16s(bflo(u) * u2f(fs[s][1]), bfhi(u) * u2f(fs[s][3]));
      *(u32*)(smem + CL_UR + c * CV_UR + 4092 - 1024 * s - 4 * pt) = pk_f16(bfhi(u), bflo(u));
      *(u32*)(smem + CL_XR + c * 4096 + 1024 * s + 4 * pt) = cx[s][c];
    }
    SB0();
  };
  const int eoff = (pw & 1) * 4096 + (512 * (pw >> 1) + 256 * (g & 1) + 16 * r16 + 4 * (g & 2)) * 2;
  u32 kiu;
  u32x4 o;
  auto put = [&](int s) AI {
    asm volatile("global_store_dwordx4 %0, %1, %2 sc1\n\ts_nop 1" ::"v"(eoff + (s >> 1) * 2048), "v"(o), "s"(xo) : "memory");
  };
  auto epi = [&](int s) AI {
    if ((pw >> 1) != (s & 1)) return;
    u32 P[2][2];
    const u32x4 xr = *(const u32x4*)(smem + CL_XR + eoff + (s >> 1) * 2048);
#pragma unroll
    for (int y = 0; y < 2; ++y) {
      const char* rp = smem + CL_RED + (s & 1) * 8192 + (pw & 1) * 4096 + y * 1024 + lane * 16;
      const f32x4 a = *(const f32x4*)rp, b = *(const f32x4*)(rp + 2048);
      const float ki = u2f(kiu);
      P[y][0] = pk_bf16s(op1(a[0] + b[0]) * ki, op1(a[1] + b[1]) * ki);
      P[y][1] = pk_bf16s(op1(a[2] + b[2]) * ki, op1(a[3] + b[3]) * ki);
    }
    swap16(P[0][0], P[1][0]);
    swap16(P[0][1], P[1][1]);
#pragma unroll
    for (int i = 0; i < 4; ++i) {
      const u32 y = P[i >> 1][i & 1], x = xr[i];
      o[i] = pk_bf16s(bflo(y) + bflo(x), bfhi(y) + bfhi(x));
    }
    if (s > 1) put(s);
    SB0();
  };
  std::integral_constant<int, 0> s0;
  std::integral_constant<int, 1> s1;
  std::integral_constant<int, 2> s2;
  std::integral_constant<int, 3> s3;
  gld4s<0>(kiu, 0u, kip);
  issue(s0);
  issue(s1, layer);
  mid();
  STAMP(2);
  asm volatile("s_waitcnt vmcnt(3)\n\ts_cmp_eq_u32 %0, 0\n\ts_cbranch_scc1 1f\n\ts_waitcnt vmcnt(0)\n1:" ::"s"(layer - red) : "scc", "memory");
  tie(kiu);
  norm(s0);
  STAMP(3);
  raw_barrier();
  issue(s2);
  wait_vm<3>();
  norm(s1);
  STAMP(4);
  raw_barrier();
  issue(s3);
  epi(0);
  wait_vm<3>();
  norm(s2);
  STAMP(5);
  raw_barrier();
  epi(1);
  put(0);
  wait_vm<1>();
  norm(s3);
  STAMP(9);
  raw_barrier();
  epi(2);
  raw_barrier();
  STAMP(6);
  epi(3);
}

template <int OFF>
DEVI void gld16(bf16x8& dst, u32 voff, const char* sbase) {
  asm volatile("global_load_dwordx4 %0, %1, %2 offset:%3" : "=v"(dst) : "v"(voff), "s"(sbase), "i"(OFF) : "memory");
}
#define G2_PF 6
#define W3_SLEEP 9
#define PACE 2
#define POLL_SLEEP 2
#define HFL "sc1"
constexpr int HNS = 12, HR0 = 160;
typedef u32 u32x8 __attribute__((ext_vector_type(8)));
struct HWin { u32x8 f[HNS]; u32 v0, v1, t, thr, cnt, dbg; const char* base; };
template <int S> struct HS;
#define LDH(S, REG) \
  template <> struct HS<S> { \
    template <int OFF> static DEVI void ld0(u32x8& dst, u32 voff, const char* sbase) { \
      asm volatile("global_load_dwordx4 v[%3:%3+3], %1, %2 offset:%4 " HFL : "={" REG "}"(dst) : "v"(voff), "s"(sbase), "i"(HR0 + 8 * S), "i"(OFF) : "memory"); } \
    template <int OFF> static DEVI void ld1(u32x8& dst, u32 voff, const char* sbase) { \
      asm volatile("global_load_dwordx4 v[%3+4:%3+7], %1, %2 offset:%4 " HFL : "+{" REG "}"(dst) : "v"(voff), "s"(sbase), "i"(HR0 + 8 * S), "i"(OFF) : "memory"); } \
    static DEVI void def(u32x8& dst) { asm volatile("" : "={" REG "}"(dst)); } \
    template <int N> static DEVI void wait(u32x8& dst) { asm volatile("s_waitcnt vmcnt(%1)" : "+{" REG "}"(dst) : "i"(N) : "memory"); } \
  };
LDH(0, "v[160:167]") LDH(1, "v[168:175]") LDH(2, "v[176:183]") LDH(3, "v[184:191]")
LDH(4, "v[192:199]") LDH(5, "v[200:207]") LDH(6, "v[208:215]") LDH(7, "v[216:223]")
LDH(8, "v[224:231]") LDH(9, "v[232:239]") LDH(10, "v[240:247]") LDH(11, "v[248:255]")
template <int J, int T>
DEVI void ld_h(HWin& h) {
  const char* b = h.base + (J & ~3) * 2048 + 4096;
  if constexpr (T == 0) HS<J % HNS>::template ld0<(J & 3) * 2048 - 4096>(h.f[J % HNS], h.v0, b);
  else HS<J % HNS>::template ld1<(J & 3) * 2048 - 4096>(h.f[J % HNS], h.v1, b);
}
template <int J, int T>
DEVI bf16x8 hfrag(HWin& h) {
  const u32x8 v = h.f[J % HNS];
  return __builtin_bit_cast(bf16x8, (u32x4){v[4 * T], v[4 * T + 1], v[4 * T + 2], v[4 * T + 3]});
}
template <int J, int K>
DEVI void chk_h(HWin& h) {
  const u32x8 v = h.f[J % HNS];
  if constexpr (K == 0) h.t = umax(umax(v[0], v[1]), v[2]);
  if constexpr (K == 1 || K == 2) h.t = umax(umax(v[2 * K + 1], v[2 * K + 2]), h.t);
  if constexpr (K == 3) asm volatile("v_max_u32 %0, %1, %0\n\tv_cmp_lt_u32 vcc, %2, %0" : "+v"(h.t) : "v"(v[7]), "s"(h.thr) : "vcc");
  if constexpr (K == 4)
    asm volatile("s_cbranch_vccnz .Lffn_s%19\n.Lffn_b%19:"
                 : "+{v[160:167]}"(h.f[0]), "+{v[168:175]}"(h.f[1]), "+{v[176:183]}"(h.f[2]), "+{v[184:191]}"(h.f[3]),
                   "+{v[192:199]}"(h.f[4]), "+{v[200:207]}"(h.f[5]), "+{v[208:215]}"(h.f[6]), "+{v[216:223]}"(h.f[7]),
                   "+{v[224:231]}"(h.f[8]), "+{v[232:239]}"(h.f[9]), "+{v[240:247]}"(h.f[10]), "+{v[248:255]}"(h.f[11]),
                   "+{v157}"(h.t), "+{s91}"(h.thr), "+{s92}"(h.cnt), "+{s85}"(h.dbg)
                 : "{s[94:95]}"(h.base), "{v158}"(h.v0), "{v159}"(h.v1), "i"(J)
                 : "vcc", "scc", "s86", "s87", "s88", "s89", "s90", "s93", "s96", "s97", "s98", "s99", "memory");
}
#define RDIAG
DEVI void reload_routine() {
#define RS(j) ".Lffn_s" #j ":\n\ts_movk_i32 s90, " #j " | (" #j " %% 12) << 8\n\ts_call_b64 s[88:89], .Lffn_reload\n\ts_branch .Lffn_b" #j "\n"
#define RL(a) "global_load_dwordx4 v[" #a ":" #a "+3], v158, s[96:97] sc1\n\tglobal_load_dwordx4 v[" #a "+4:" #a "+7], v159, s[96:97] sc1\n\ts_branch 1b\n\t"
#define RC(a) "v_max3_u32 v157, v[" #a "], v[" #a "+1], v[" #a "+2]\n\tv_max3_u32 v157, v[" #a "+3], v[" #a "+4], v157\n\t" \
              "v_max3_u32 v157, v[" #a "+5], v[" #a "+6], v157\n\tv_max_u32 v157, v[" #a "+7], v157\n\ts_branch 4f\n\t"
#define R12(M) M(160) M(168) M(176) M(184) M(192) M(200) M(208) M(216) M(224) M(232) M(240) M(248)
#define RJ "s_add_u32 vcc_lo, vcc_lo, 12\n\ts_getpc_b64 s[98:99]\n\ts_add_u32 s98, s98, vcc_lo\n\ts_addc_u32 s99, s99, 0\n\ts_setpc_b64 s[98:99]\n\t"
  asm volatile("s_branch 2f\n"
               RS(0) RS(1) RS(2) RS(3) RS(4) RS(5) RS(6) RS(7) RS(8) RS(9) RS(10)
               RS(11) RS(12) RS(13) RS(14) RS(15) RS(16) RS(17) RS(18) RS(19) RS(20) RS(21)
               ".Lffn_reload:\n\t"
               "s_sub_u32 s92, s92, 1\n\ts_cselect_b32 s91, -1, s91\n\t" RDIAG
               "s_and_b32 s93, s90, 0xff\n\ts_lshr_b32 s86, s90, 8\n\ts_add_u32 s87, s93, %1\n\ts_min_u32 s87, s87, 22\n\t"
               "s_sleep %0\n"
               "1:\n\ts_cmp_ge_u32 s93, s87\n\ts_cbranch_scc1 3f\n\t"
               "s_lshl_b32 s96, s93, 11\n\ts_add_u32 s96, s94, s96\n\ts_addc_u32 s97, s95, 0\n\t"
               "s_mul_i32 vcc_lo, s86, 20\n\ts_add_u32 s93, s93, 1\n\ts_add_u32 s86, s86, 1\n\t"
               "s_cmp_eq_u32 s86, 12\n\ts_cselect_b32 s86, 0, s86\n\t" RJ R12(RL)
               "\n3:\n\ts_lshr_b32 vcc_lo, s90, 8\n\ts_lshl_b32 vcc_lo, vcc_lo, 5\n\ts_waitcnt vmcnt(0)\n\t" RJ R12(RC)
               "\n4:\n\tv_cmp_lt_u32 vcc, s91, v157\n\ts_cbranch_vccnz .Lffn_reload\n\ts_setpc_b64 s[88:89]\n"
               "2:" ::"i"(POLL_SLEEP), "i"(G2_PF));
}
template <bool FIRST>
DEVI void mma(f32x4& c, bf16x8 a, bf16x8 b) {
  if constexpr (FIRST) asm volatile("v_mfma_f32_16x16x32_bf16 %0, %1, %2, 0" : "=&v"(c) : "v"(a), "v"(b));
  else asm volatile("v_mfma_f32_16x16x32_bf16 %0, %1, %2, %0" : "+v"(c) : "v"(a), "v"(b));
}
DEVI void gld8(u32x2& dst, u32 voff, const char* sbase) {
  asm volatile("global_load_dwordx2 %0, %1, %2 sc1" : "=v"(dst) : "v"(voff), "s"(sbase) : "memory");
}
constexpr int FF_CS = 131072;
constexpr int FF_FLG = FF_CS + 960;
constexpr int FF_CNT = FF_FLG + 48;
constexpr int FF_R1 = FF_CS + 1024;
constexpr int FF_R2 = FF_R1 + 512;
constexpr int FF_ST = FF_R2 + 2048;
constexpr int FF_OUT = FF_ST + 2048;
constexpr int FF_JUNK = FF_OUT + 9216;
constexpr int FF_END = FF_JUNK + 3072;
constexpr int FF_XR = 12 * 4096;
#define W2A 4
#define W2B 12
#define W2C 20
DEVI void set_flag(char* smem, int word, u32 val = 1u) {
  asm volatile("" ::: "memory");
  *(volatile LDS_AS u32*)(smem + FF_FLG + 4 * word) = val;
  asm volatile("" ::: "memory");
}
DEVI u32 wait_flags(char* smem, int word) {
  const u32 a = FF_FLG + 4 * word + (u32)(uintptr_t)(LDS_AS char*)smem;
  u32 v, it;
  asm volatile("s_mov_b32 %1, 0x100000\n"
               "1:\n\tds_read_b32 %0, %2\n\ts_sub_u32 %1, %1, 1\n\ts_cbranch_scc1 2f\n\ts_waitcnt lgkmcnt(0)\n\t"
               "v_cmp_eq_u32 vcc, 0, %0\n\ts_cbranch_vccnz 1b\n"
               "2:\n\ts_waitcnt lgkmcnt(0)"
               : "=&v"(v), "=&s"(it) : "v"(a) : "vcc", "scc", "memory");
  return v;
}

template <int NW>
DEVI u64 g1_epi(char* smem, const f32x4 (&acc)[3][8], const f32x4 (&c4)[NW], char* hbase, int w, int lane, int ft ST_ARG) {
  const int r16 = lane & 15, g = lane >> 4;
  f32x4 st[8];
  const u32 lds0 = (u32)(uintptr_t)(LDS_AS char*)smem;
  u64 ok[2];
  auto get_st = [&](int e) AI {
    u32 v, it;
    asm volatile("s_mov_b32 %5, 0x100000\n"
                 "1:\n\tds_read_b32 %0, %7\n\tds_read_b128 %1, %8\n\tds_read_b128 %2, %8 offset:256\n\t"
                 "ds_read_b128 %3, %8 offset:512\n\tds_read_b128 %4, %8 offset:768\n\t"
                 "s_sub_u32 %5, %5, 1\n\ts_cbranch_scc1 2f\n\ts_waitcnt lgkmcnt(4)\n\t"
                 "v_cmp_eq_u32 vcc, 0, %0\n\ts_cbranch_vccnz 1b\n"
                 "2:\n\ts_waitcnt lgkmcnt(0)\n\tv_cmp_ne_u32 %6, 2, %0"
                 : "=&v"(v), "=&v"(st[4 * e]), "=&v"(st[4 * e + 1]), "=&v"(st[4 * e + 2]), "=&v"(st[4 * e + 3]), "=&s"(it), "=&s"(ok[e])
                 : "v"(lds0 + FF_FLG + 16 + 4 * e), "v"(lds0 + FF_ST + e * 1024 + r16 * 16)
                 : "vcc", "scc", "memory");
  };
  get_st(0);
  STAMP(6);
  const u32 hv = (g >> 1) * (NKS2 * 2048) + (g & 1) * 1024 + r16 * 16;
#pragma unroll
  for (int i = 0; i < NW; ++i)
#pragma unroll
    for (int e = 0; e < 2; ++e) {
      f32x2 a[4], b[4], x[4];
      SB0();
      if (e == 1 && i == 0) get_st(1);
      SB0();
#pragma unroll
      for (int jj = 0; jj < 4; ++jj) {
        const int j = 4 * e + jj;
        a[jj] = __builtin_elementwise_fma((f32x2){st[j][0], st[j][0]}, (f32x2){c4[i][0], c4[i][1]}, (f32x2){acc[i][j][0], acc[i][j][1]});
      }
      SB0();
#pragma unroll
      for (int jj = 0; jj < 4; ++jj) x[jj] = a[jj] * st[4 * e + jj][1];
      SB0();
#pragma unroll
      for (int jj = 0; jj < 4; ++jj) {
        const int j = 4 * e + jj;
        x[jj][0] = __builtin_amdgcn_exp2f(x[jj][0]);
        SB0();
        b[jj] = __builtin_elementwise_fma((f32x2){st[j][0], st[j][0]}, (f32x2){c4[i][2], c4[i][3]}, (f32x2){acc[i][j][2], acc[i][j][3]});
        SB0();
        x[jj][1] = __builtin_amdgcn_exp2f(x[jj][1]);
        SB0();
      }
#pragma unroll
      for (int jj = 0; jj < 4; ++jj) {
        const f32x2 k2 = {st[4 * e + jj][2], st[4 * e + jj][2]};
        x[jj] = __builtin_elementwise_fma(x[jj], k2, k2);
      }
      SB0();
#pragma unroll
      for (int jj = 0; jj < 4; ++jj) {
        x[jj][0] = __builtin_amdgcn_rcpf(x[jj][0]);
        SB0();
        a[jj] = a[jj] * b[jj];
        SB0();
        x[jj][1] = __builtin_amdgcn_rcpf(x[jj][1]);
        SB0();
      }
      u32 P[4];
#pragma unroll
      for (int jj = 0; jj < 4; ++jj) a[jj] = a[jj] * x[jj];
      SB0();
#pragma unroll
      for (int jj = 0; jj < 4; ++jj) P[jj] = pk_bf16(a[jj][0], a[jj][1]);
      swap32(P[0], P[2]); swap32(P[1], P[3]);
      swap16(P[0], P[1]); swap16(P[2], P[3]);
      const int Rg = 88 * (ft >> 3) + (i < 2 ? 32 * i + 4 * (ft & 7) : 64 + 3 * (ft & 7)) + w;
      const char* dst = hbase + (Rg >> 2) * 2048 + (Rg & 3) * 256;
      const u32x4 pv = {P[0], P[1], P[2], P[3]};
      asm volatile("s_mov_b64 exec, %3\n\tglobal_store_dwordx4 %0, %1, %2 sc1\n\ts_mov_b64 exec, -1\n\ts_nop 0"
                   ::"v"(hv + e * (NKS2 * 4096)), "v"(pv), "s"(dst), "s"(ok[e]) : "memory");
      SB0();
    }
  return ok[0] & ok[1];
}

template <bool W3>
DEVI u64 g1_main(char* smem, bf16x8 (&w0)[3][3], const char* wbase, char* hbase, const char* xrb, u32x2 (&xres)[4],
                  int wv, int lane, int ft ST_ARG) {
  constexpr int NW = W3 ? 2 : 3;
  const int r16 = lane & 15, g = lane >> 4;
  const u32 lane16 = lane * 16;
  bf16x8 wf[16][NW];
  auto ld_w = [&](auto K, auto I) AI {
    constexpr int k = K, i = I;
    gld16<i * 1024>(wf[k][i], lane16, wbase + k * 3072);
  };
  const int q = r16 >> 2, p = lane & 3;
  const int hl = q | ((g & 1) << 2);
  int raddr[8];
#pragma unroll
  for (int j = 0; j < 8; ++j) raddr[j] = (8 * g + q) * 256 + 8 * p + ((j ^ hl) * 32);
  f32x4 acc[3][8], c4[NW];
  u32x2 ulo[2][8], uhi[2][8];
  auto rd_lo = [&](int s, int f) AI { ulo[s & 1][f] = tr_read(smem + raddr[f] + (32 * (s & 7)) * 256); };
  auto rd_hi = [&](int s, int f) AI { uhi[s & 1][f] = tr_read(smem + raddr[f] + (32 * (s & 7) + 4) * 256); };
  auto uf = [&](int s, int f) AI {
    return __builtin_bit_cast(bf16x8, (u32x4){ulo[s & 1][f][0], ulo[s & 1][f][1], uhi[s & 1][f][0], uhi[s & 1][f][1]});
  };
  auto mm = [&](int i, int s, int f) AI {
    acc[i][f] = __builtin_amdgcn_mfma_f32_16x16x32_bf16((W3 && i == 2) ? uf(s, f) : wf[s][i < NW ? i : 0], uf(s, f), acc[i][f], 0, 0, 0);
    SB0();
  };
  const int a1 = g == 3 ? FF_R1 + r16 * 4 : FF_JUNK + lane * 4;
  const int a2 = g == q ? FF_R2 + r16 * 16 : FF_JUNK + lane16;
  auto st_s = [&](int f) AI {
    *(float*)(smem + a1 + f * 64) = acc[1][f][0];
    *(f32x4*)(smem + a2 + f * 256) = acc[2][f];
    SB0();
  };
  auto rd_c = [&](int i) AI { c4[i] = *(const f32x4*)(smem + FF_CS + ((3 * wv + i) * 16 + 4 * g) * 4); SB0(); };
#pragma unroll
  for (int i = 0; i < 3; ++i)
#pragma unroll
    for (int j = 0; j < 8; ++j) acc[i][j] = (f32x4){0.f, 0.f, 0.f, 0.f};
  STAMP(2);
  bare_barrier();
  STAMP(3);
#pragma unroll
  for (int f = 0; f < 8; ++f) { rd_lo(0, f); rd_hi(0, f); }
  static_for<0, 16>([&](auto S) AI {
    constexpr int s = S;
    constexpr bool late = W3 && s >= 12;
    SB0();
    if constexpr (s < 3) { if constexpr (s == 1) wait_vm<4 + NW>(); }
    else if constexpr (W3) wait_vm<(s < 11 ? 2 * NW : (s < 13 ? 3 * NW : (s == 13 ? 2 * NW : (s == 14 ? NW + 4 : 4))))>();
    else wait_vm<(s < 14 ? 2 * NW : (s == 14 ? NW + 4 : 4))>();
    if constexpr (W3 && s == 0) { tie(w0[0][2]); tie(w0[1][2]); tie(w0[2][2]); }
#pragma unroll
    for (int i = 0; i < NW; ++i) {
      if constexpr (s < 3) { tie(w0[s < 3 ? s : 0][i]); wf[s][i] = w0[s < 3 ? s : 0][i]; }
      else tie(wf[s][i]);
    }
    SB0();
    static_for<0, 8>([&](auto F) AI {
      constexpr int f = F;
      if constexpr (!late) mm(0, s, f);
      if constexpr (f == 1 && (s & 1) && s < 12) { bare_barrier(); SB0(); }
      if constexpr (s == 7) { raddr[f] += 65536; tie(raddr[f]); }
      if constexpr (s < 15 && f >= 1) rd_lo(s + 1, f - 1);
      SB0();
      mm(1, s, f);
      if constexpr (s < 15 && f >= 1) rd_hi(s + 1, f - 1);
      SB0();
      mm(2, s, f);
      if constexpr (f >= 2 && f < 2 + NW && s < (W3 ? 11 : 13))
        ld_w(std::integral_constant<int, (s < 13 ? s + 3 : 0)>{}, std::integral_constant<int, (f >= 2 && f < 2 + NW ? f - 2 : 0)>{});
      if constexpr (W3 && (s == 10 || s == 11) && f >= 5 && f < 7)
        ld_w(std::integral_constant<int, (s < 12 ? s + 4 : 0)>{}, std::integral_constant<int, (f >= 5 && f < 7 ? f - 5 : 0)>{});
      if constexpr (W3 && s == 15 && f >= 2) st_s(f - 2);
      if constexpr (W3 && s == 15 && f == 5) set_flag(smem, 0);
      if constexpr (!W3 && s == 15 && f < NW) rd_c(f);
      if constexpr (s == 13 && f >= 2 && f < 6) gld8(xres[f - 2], r16 * (L_ * 2) + g * 8, xrb + (size_t)(f - 2) * (16 * L_ * 2));
      if constexpr (s < 15 && f == 7) { rd_lo(s + 1, 7); rd_hi(s + 1, 7); }
      SB0();
    });
  });
  if constexpr (W3) {
    static_for<0, 8>([&](auto F) AI {
      constexpr int f = F;
      mm(0, 14, f);
      if constexpr (f == 0 || f == 2) st_s(6 + f / 2);
      if constexpr (f == 2) set_flag(smem, 1);
      if constexpr (f >= 1) { rd_lo(12, f - 1); rd_hi(12, f - 1); }
      SB0();
    });
    rd_lo(12, 7); rd_hi(12, 7);
    static_for<0, 8>([&](auto F) AI {
      constexpr int f = F;
      mm(0, 15, f);
      if constexpr (f >= 1) { rd_lo(13, f - 1); rd_hi(13, f - 1); }
      SB0();
    });
    rd_lo(13, 7); rd_hi(13, 7);
    rd_c(0); rd_c(1);
    static_for<0, 16>([&](auto F) AI { mm(0, 12 + (F >> 3), F & 7); });
  }
  set_flag(smem, 8 + wv);
  STAMP(5);
  const u64 ok = g1_epi<NW>(smem, acc, c4, hbase, wv, lane, ft ST_PASS);
  if constexpr (W3 && W3_SLEEP > 0) __builtin_amdgcn_s_sleep(W3_SLEEP);
  return ok;
}

struct Tile { int mt, ft, lh, nq, h2, nt, l0, d0; };

DEVI u64 ffn_m1(char* smem, bf16x8 (&w0)[3][3], const char* wbase, const char* xT, char* hbase, u32x2 (&xres)[4], const Tile& T,
                 int wv, int lane ST_ARG) {
  const char* xrb = xT + ((size_t)(64 * T.nt) * L_ + T.l0 + 16 * (2 * (wv & 1) + (wv >> 1))) * 2;
  u64 ok = 0;
  if (wv == 3) {
    ok = g1_main<true>(smem, w0, wbase, hbase, xrb, xres, wv, lane, T.ft ST_PASS);
#pragma unroll
    for (int q = 0; q < 9; ++q) asm volatile("" : "=v"(w0[q / 3][q % 3]));
    asm volatile("" : "=v"(lane));
  }
  ties(wv);
  if (wv != 3) ok = g1_main<false>(smem, w0, wbase, hbase, xrb, xres, wv, lane, T.ft ST_PASS);
  return ok;
}

DEVI void ffn_m2(char* smem, const char* hbase, u32x2 (&xres)[4], int lh, int wv, int lane, u32 mb ST_ARG) {
  const int r16 = lane & 15, g = lane >> 4;
  const u32 lane16 = lane * 16;
  const int kh = wv >> 1, rh = wv & 1, rt = 2 * rh + kh;
  HWin hw;
  hw.base = hbase + ((2 * lh + rh) * NKS2 + 22 * kh) * 2048;
  hw.v0 = lane16 + (1 - kh) * 1024;
  hw.v1 = lane16 + kh * 1024;
  hw.thr = 0xfffffffeu;
  hw.cnt = 0x4000u;
  hw.dbg = 0u;
  STAMP(7);
  static_for<0, G2_PF>([&](auto J) AI { ld_h<J, 0>(hw); ld_h<J, 1>(hw); });
  static_for<G2_PF, HNS>([&](auto J) AI { HS<J>::def(hw.f[J]); });
  int lo16 = lane16 + kh * 4096, hi16 = lo16 + 65536;
  tie(lo16); tie(hi16);
  bare_barrier();
  STAMP(9);
  bf16x8 wfr[2][4];
  const char* wl0 = smem + lo16;
  const char* wl1 = smem + hi16;
  auto rd_w = [&](int j, int b2) AI {
    const int sl = j & 15;
    wfr[j & 1][b2] = *(const bf16x8*)((sl < 8 ? wl0 : wl1) + (sl & 7) * 8192 + b2 * 1024);
  };
#pragma unroll
  for (int j = 0; j < 2; ++j)
#pragma unroll
    for (int b2 = 0; b2 < 4; ++b2) rd_w(j, b2);
  f32x4 acc[2][4];
  char* xw = smem + FF_XR + wv * 4096 + lane16;
  SB0();
  HS<0>::wait<2 * G2_PF - 2>(hw.f[0]);
  static_for<0, 5>([&](auto K) AI { chk_h<0, K>(hw); });
#pragma unroll
  for (int b2 = 0; b2 < 4; ++b2) tie(xres[b2]);
  STAMP(10);
  static_for<0, 22>([&](auto J) AI {
    constexpr int j = J;
    constexpr bool ld = j + G2_PF < 22;
    constexpr int jl = ld ? j + G2_PF : 0, jn = j < 21 ? j + 1 : 0;
    SB0();
    if constexpr (j == 6 || j == 14) bare_barrier();
    __builtin_amdgcn_s_waitcnt(j < 21 ? 0xc47f : 0xc07f);
    SB0();
    if constexpr (j < 21) {
      static_for<0, 4>([&](auto B) AI {
        constexpr int b2 = B;
        const bf16x8 wb = wfr[j & 1][b2];
        mma<j == 0>(acc[0][b2], hfrag<j, 0>(hw), wb);
        SB0();
        if constexpr (ld && b2 < 2) ld_h<jl, (b2 & 1)>(hw);
        if constexpr (b2 == 0) HS<jn % HNS>::template wait<ld ? 2 * G2_PF - 3 : 2 * (21 - jn)>(hw.f[jn % HNS]);
        if constexpr (b2 == 1 || b2 == 2) chk_h<jn, 2 * b2 - 1>(hw);
        if constexpr (b2 == 3) chk_h<jn, 4>(hw);
        SB0();
        if constexpr (j + 2 < 22 && b2 == 3) rd_w(j + 2, b2);
        SB0();
        mma<j == 0>(acc[1][b2], hfrag<j, 1>(hw), wb);
        SB0();
        if constexpr (j + 2 < 22 && b2 < 3) rd_w(j + 2, b2);
        if constexpr (b2 < 2) chk_h<jn, 2 * b2>(hw);
        SB0();
      });
    } else {
#pragma unroll
      for (int b2 = 0; b2 < 4; ++b2) {
        acc[0][b2] = __builtin_amdgcn_mfma_f32_16x16x32_bf16(hfrag<j, 0>(hw), wfr[j & 1][b2], acc[0][b2], 0, 0, 0);
        SB0();
      }
#pragma unroll
      for (int b2 = 0; b2 < 4; ++b2) {
        acc[1][b2] = __builtin_amdgcn_mfma_f32_16x16x32_bf16(hfrag<j, 1>(hw), wfr[j & 1][b2], acc[1][b2], 0, 0, 0);
        SB0();
        *(f32x4*)(xw + b2 * 1024) = acc[0][b2];
        SB0();
      }
    }
  });
  STAMP(11);
  raw_barrier();
  {
    const char* xr = smem + FF_XR + (wv ^ 2) * 4096 + lane16;
    f32x4 pa[4];
#pragma unroll
    for (int b2 = 0; b2 < 4; ++b2) pa[b2] = *(const f32x4*)(xr + b2 * 1024);
    SB0();
#pragma unroll
    for (int b2 = 0; b2 < 4; ++b2) {
      const f32x4 a = acc[1][b2] + pa[b2];
      u32x2 ov;
#pragma unroll
      for (int h = 0; h < 2; ++h) {
        const f32x2 y = rbf2((f32x2){a[2 * h], a[2 * h + 1]}) + (f32x2){bflo(xres[b2][h]), bfhi(xres[b2][h])};
        ov[h] = pk_bf16(y[0], y[1]);
      }
      *(u32x2*)(smem + FF_OUT + (16 * b2 + r16) * 144 + 32 * rt + 8 * g) = ov;
    }
  }
  raw_barrier();
  STAMP(12);
}

DEVI void x_chunk(const XT& x, int c) {
#pragma unroll
  for (int n = 0; n < 4; ++n) glds16s((n & 2) ? x.xv ^ 128 : x.xv, x.lds + c * 16384 + n * 1024, x.xb + (size_t)(64 * c + 4 * n) * (L_ * 2));
}
DEVI void x_start(const XT& x, u32 (&pr)[2], int& need, u32& budget) {
  do {
    if (EARLY_LQ == 0 || need) {
      gld4s<252>(pr[0], x.pa, x.xb);
      x_chunk(x, 0);
      gld4s<252>(pr[1], x.pb, x.xb);
      x_chunk(x, 1);
      x_chunk(x, 2);
    }
    asm volatile("s_mov_b32 %0, 1" : "=s"(need));
    wait_vm<8>();
    tie(pr[0]); tie(pr[1]);
  } while (again(__builtin_amdgcn_ballot_w64(umax(pr[0], pr[1]) == ~0u), budget));
}
DEVI const char* w2_base(const char* ws, const Tile& T, int kh, int lw) {
  const char* p = ws + OFF_W2 + ((size_t)(T.h2 * NKS2 + 22 * kh) * 16 + 4 * T.nq + lw) * 1024;
  ties(p);
  return p;
}

DEVI u32 ffn_h1(char* smem, const char* ws, const char* xT, bf16x8 (&wr)[12], u32& budget, const Tile& T,
                int lw, int lane ST_ARG) {
  const int ft = T.ft;
  const int r16 = lane & 15, g = lane >> 4;
  const u32 lane16 = lane * 16;
  const u32 lds0 = (u32)(uintptr_t)(LDS_AS char*)smem;
  if (lw == 0) *(u32*)(smem + FF_FLG + r16 * 4) = 0u;
  const XT xt = x_tile(smem, xT, T.mt, lw, lane);
  const char* wsg[2] = {w2_base(ws, T, 0, lw), w2_base(ws, T, 1, lw)};
  const u32 ldw = lds0 + lw * 1024;
  auto wfill = [&](int s0, int s1) AI {
#pragma unroll
    for (int sl = s0; sl < s1; ++sl) glds16(lane16, ldw + sl * 4096, wsg[sl & 1] + (size_t)(sl >> 1) * 16384);
  };
  static_for<0, 5>([&](auto C) AI {
    if constexpr (C >= 3) wait_vm<8>();
    if constexpr (C == 0) STAMP(2);
    if constexpr (C == 0) raw_barrier();
    else bare_barrier();
    x_chunk(xt, C + 3);
  });
  bf16x8 cp[4];
#pragma unroll
  for (int u = 0; u < 4; ++u)
    gld16<0>(cp[u], (4 * g * (16 * 44) + 11 * lw + (r16 < 11 ? r16 : 0)) * 16, ws + OFF_CS + ft * 704 + u * (16 * 704));
  wfill(0, W2A);
  wait_vm<12 + W2A>();
  bare_barrier();
  wfill(W2A, W2B);
  wait_vm<W2B>();
  tie(cp[0]); tie(cp[1]); tie(cp[2]); tie(cp[3]);
  {
    u32 c[4];
#pragma unroll
    for (int i = 0; i < 4; ++i) {
      float t = op1(__builtin_bit_cast(f32x4, cp[0])[i] + __builtin_bit_cast(f32x4, cp[1])[i]) +
                op1(__builtin_bit_cast(f32x4, cp[2])[i] + __builtin_bit_cast(f32x4, cp[3])[i]);
      tie(t);
      c[i] = __builtin_bit_cast(u32, t);
    }
    swap32(c[0], c[1]); swap32(c[2], c[3]);
    u32 y = __builtin_bit_cast(u32, __builtin_bit_cast(float, c[0]) + __builtin_bit_cast(float, c[1]));
    tie(y);
    u32 z = __builtin_bit_cast(u32, __builtin_bit_cast(float, c[2]) + __builtin_bit_cast(float, c[3]));
    swap16(y, z);
    const float t = __builtin_bit_cast(float, y) + __builtin_bit_cast(float, z);
    int ca = FF_CS + (11 * lw + r16) * 16 + (2 * (g & 1) + (g >> 1)) * 4, cb = FF_JUNK + lane * 4;
    tie(ca); tie(cb);
    *(float*)(smem + (r16 < 11 ? ca : cb)) = t;
  }
  raw_barrier();
  wfill(W2B, W2C);
  STAMP(5);
  if (lw < 2) {
    __builtin_amdgcn_s_setprio(3);
    const int row = 64 * lw + lane;
    float s1, s2;
    {
      u32 v, it;
      asm volatile("s_mov_b32 %3, 0x100000\n"
                   "1:\n\tds_read_b32 %0, %4\n\tds_read_b32 %1, %5\n\tds_read_b32 %2, %6\n\t"
                   "s_sub_u32 %3, %3, 1\n\ts_cbranch_scc1 2f\n\ts_waitcnt lgkmcnt(0)\n\t"
                   "v_cmp_eq_u32 vcc, 0, %0\n\ts_cbranch_vccnz 1b\n"
                   "2:\n\ts_waitcnt lgkmcnt(0)"
                   : "=&v"(v), "=&v"(s1), "=&v"(s2), "=&s"(it)
                   : "v"(lds0 + FF_FLG + 4 * lw), "v"(lds0 + FF_R1 + row * 4), "v"(lds0 + FF_R2 + row * 16 + (lane & 3) * 4)
                   : "vcc", "scc", "memory");
    }
    const float mean = s1 * (1.0f / D_);
    const float var = fmaxf(fmaf(-mean, mean, s2 * (1.0f / D_)), 0.f);
    const float sd = rbf(__builtin_amdgcn_sqrtf(rbf(rbf(var) + 1e-5f)));
    *(f32x4*)(smem + FF_ST + row * 16) = (f32x4){-rbf(mean), op1(-1.44269504f * __builtin_amdgcn_rcpf(sd)), op1(sd * sd), 0.f};
    u32 fv;
    asm volatile("s_cmp_lg_u64 %2, 0\n\ts_cselect_b32 %0, %1, 0\n\ts_min_u32 %0, %0, 0x100\n\ts_sub_u32 %1, %1, %0\n\t"
                 "s_cmp_lg_u32 %0, 0\n\ts_cselect_b32 %0, 2, 1"
                 : "=&s"(fv), "+s"(budget) : "s"(__builtin_amdgcn_ballot_w64(__builtin_isunordered(s1, s2))) : "scc");
    set_flag(smem, 4 + lw, fv);
    __builtin_amdgcn_s_setprio(0);
  }
  STAMP(6);
  static_for<0, 12>([&](auto I) AI {
    constexpr int t = I;
    gld16<0>(wr[t], lane16, wsg[t & 1] + (size_t)(16 + (t >> 1)) * 16384);
  });
  STAMP(7);
  const u32 fl = wait_flags(smem, (lane & 4) ? 4 + (lane & 1) : 8 + (lane & 3));
  STAMP(8);
  u32 redo;
  asm volatile("s_cmp_lg_u64 %1, 0\n\ts_cselect_b32 %0, 1, 0\n\ts_cbranch_scc0 1f\n\ts_waitcnt vmcnt(0)\n\ts_barrier\n1:"
               : "=s"(redo) : "s"(__builtin_amdgcn_ballot_w64(fl == 2u)) : "scc", "memory");
  return redo;
}

DEVI void ffn_h2(char* smem, const char* ws, bf16x8 (&wr)[12], const char* xp, int layer, const Tile& T, int lw, int lane ST_ARG) {
  const u32 lane16 = lane * 16;
  const char* wsg[2] = {w2_base(ws, T, 0, lw), w2_base(ws, T, 1, lw)};
  const u32 ldw = (u32)(uintptr_t)(LDS_AS char*)smem + lw * 1024;
  static_assert(W2C >= 16, "");
#pragma unroll
  for (int sl = W2C; sl < 32; ++sl) {
    glds16(lane16, ldw + sl * 4096, wsg[sl & 1] + (size_t)(sl >> 1) * 16384);
    __builtin_amdgcn_s_sleep(PACE);
  }
  reload_routine();
  wait_vm<28>();
  STAMP(4);
  bare_barrier();
  STAMP(9);
  wait_vm<0>();
  bare_barrier();
#pragma unroll
  for (int t = 0; t < 12; ++t) tie(wr[t]);
#pragma unroll
  for (int t = 0; t < 12; ++t) *(bf16x8*)(smem + t * 4096 + lw * 1024 + lane16) = wr[t];
  {
    const u32x4 ones = all_ones();
    asm volatile("s_cmp_lg_u32 %3, 0\n\ts_cbranch_scc1 1f\n\tglobal_store_dwordx4 %0, %1, %2 offset:-2048 sc1\n\t"
                 "global_store_dwordx4 %0, %1, %2 offset:2048 sc1\n\ts_nop 1\n1:"
                 ::"v"(lane16), "v"(ones), "s"(xp), "s"(layer) : "scc", "memory");
  }
  raw_barrier();
  bare_barrier();
  wait_vm<0>();
  bare_barrier();
  STAMP(12);
}

#define RED_LQ 2
#define NAP_R 6
#define NAP_C 0x00001010
#define NAP_A 0x10090404
#define NAP_B 0x00081010
DEVI Tile tile_of(int b) {
  const int hf = b & 1, lq = (b >> 1) & 3, idx = b >> 3, j8 = idx >> 2;
  Tile T;
  T.mt = 4 * lq + (idx & 3); T.ft = 8 * hf + j8;
  T.lh = j8 & 1; T.nq = j8 >> 1; T.h2 = hf;
  T.nt = 4 * T.h2 + T.nq;
  T.l0 = 128 * T.mt + 64 * T.lh;
  T.d0 = chan_of_block(b);
  return T;
}
DEVI u32x4 out_frag(const char* smem, int lane, int lb, int kk) {
  const char* src = smem + FF_OUT + (32 * kk + 8 * (lane >> 4) + ((lane & 15) >> 2)) * 144 + lb * 32 + (lane & 3) * 8;
  const u32x2 lo = tr_read(src), hi = tr_read(src + 4 * 144);
  return (u32x4){lo[0], lo[1], hi[0], hi[1]};
}
__global__ __launch_bounds__(512)
void main_kernel(char* __restrict__ ws0, u16* __restrict__ out) {
  __shared__ __attribute__((aligned(1024))) char smem[CL_END + DIAG_LDS];
  STAMP_BEGIN(CL_END);
  const int wv0 = __builtin_amdgcn_readfirstlane(threadIdx.x >> 6);
  int lane, bid, wv;
  char* ws;
  auto fresh = [&]() AI {
    asm volatile("v_mbcnt_lo_u32_b32 %0, -1, 0\n\tv_mbcnt_hi_u32_b32 %0, -1, %0" : "=v"(lane));
    bid = blockIdx.x; wv = wv0; ws = ws0;
    ties(bid); ties(wv); ties(ws);
  };
  if (wv0 < 4) {
    fresh();
    KRegs k;
    k_load(k, ws, chan_of_block(bid), wv, lane);
    *(u32*)(smem + CL_UR + (wv >> 1) * CV_UR + 4096 + 256 * (wv & 1) + lane * 4) = 0u;
    u32 budget = 0x4000u;
    int layer = 0;
#pragma nounroll
    do {
      bf16x8 w0[3][3];
      u32x2 xres[4];
      auto wbase = [&]() AI { return ws + OFF_W13 + (size_t)(tile_of(bid).ft * 4 + wv) * 49152; };
      auto hbase = [&]() AI { return ws + OFF_H + layer * HBYTES + (size_t)tile_of(bid).mt * (NKS2 * 8192); };
      fresh();
      u32 pr[2];
      int need = (bid & 6) + 8 - 2 * EARLY_LQ;
      ties(need);
      need >>= 3;
      ties(need);
      conv_m(smem, k, w0, pr, wbase(), ws + OFF_H + layer * HBYTES + bid * 8192 + wv * 2048,
             ws + OFF_H + layer * HBYTES + 2097152 + bid * 14336 + wv * 1024, ws + OFF_X0 + 2 * XBYTES + bid * 8192 + wv * 2048,
             ws + OFF_STP + bid * 512 + wv * 128, ws + OFF_X0 + (2 * layer + 1) * XBYTES, tile_of(bid).mt, need, layer, wv,
             lane ST_PASS);
      STAMP(12); STAMP_NEXT();
      nap((NAP_A >> (4 * (bid & 6))) & 255);
      u64 ok;
#pragma nounroll
      do {
        fresh();
        x_start(x_tile(smem, ws + OFF_X0 + (2 * layer + 1) * XBYTES, tile_of(bid).mt, wv, lane), pr, need, budget);
        fresh();
        ok = ffn_m1(smem, w0, wbase(), ws + OFF_X0 + (2 * layer + 1) * XBYTES, hbase(), xres, tile_of(bid), wv, lane ST_PASS);
        fresh();
#define WL(q, o) "global_load_dwordx4 %" #q ", %10, %12 offset:" #o "\n\t"
        asm volatile("s_cmp_lg_u64 %9, 0\n\ts_cbranch_scc1 1f\n\ts_waitcnt vmcnt(0)\n\ts_barrier\n\t"
                     WL(0, -4096) WL(1, -3072) WL(2, -2048) WL(3, -1024) WL(4, 0) WL(5, 1024) WL(6, 2048) WL(7, 3072)
                     "global_load_dwordx4 %8, %11, %12 offset:3072\n\ts_waitcnt vmcnt(0)\n1:"
                     : "=&v"(w0[0][0]), "=&v"(w0[0][1]), "=&v"(w0[0][2]), "=&v"(w0[1][0]), "=&v"(w0[1][1]), "=&v"(w0[1][2]),
                       "=&v"(w0[2][0]), "=&v"(w0[2][1]), "=&v"(w0[2][2]), "+s"(ok)
                     : "v"(lane * 16), "v"(lane * 16 + 1024), "s"(wbase() + 4096) : "scc", "memory");
      } while (ok == 0);
      fresh();
      ffn_m2(smem, hbase(), xres, tile_of(bid).lh, wv, lane, 0x4001u - budget ST_PASS);
#pragma unroll
      for (int h = 0; h < 3; ++h) asm volatile("" : "=v"(k.a[h]), "=v"(k.pr[h]));
      if (layer == 0) {
        fresh();
        const Tile T = tile_of(bid);
        const u32 so = (bid & 4) ? (u32)OFF_STP + T.nt * (L_ * 8) + (T.l0 + 16 * wv) * 8 : (u32)OFF_P0 + ((T.l0 >> 4) + wv) * 8192 + T.nt * 128;
        row_stats(ws + so, bid & 4, out_frag(smem, lane, wv, 0), out_frag(smem, lane, wv, 1), lane);
        k_load(k, ws, T.d0, wv, lane);
        STAMP(13); STAMP_NEXT();
      }
      ++layer;
      ties(layer);
    } while (layer < 2);
  } else {
    u32 budget = 0x4000u;
    int layer = 0;
#pragma nounroll
    do {
      bf16x8 wr[12];
      auto reduce = [&]() AI {
        const Tile T = tile_of(bid);
        __builtin_amdgcn_s_sleep(NAP_R);
        u32x2 q;
        do {
          gld8(q, (lane >> 3) * (L_ * 8) + (lane & 7) * 8, ws + OFF_STP + (T.l0 + 8 * T.nt) * 8);
          wait_vm<0>();
          tie(q);
        } while (again(__builtin_amdgcn_ballot_w64(umax(q[0], q[1]) == ~0u), budget));
        STAMP(7);
        u32 a = f2u(dpp_add<0x128>(u2f(q[0]))), b = f2u(dpp_add<0x128>(u2f(q[1])));
        swap16(a, b);
        a = f2u(op1(u2f(a) + u2f(b)));
        b = a;
        swap32(a, b);
        a = f2u(op1(u2f(a) + u2f(b)));
        b = a;
        swap16(a, b);
        const f32x2 f = ln_final(u2f(a), u2f(b));
        asm volatile("global_store_dwordx2 %0, %1, %2 sc1" ::"v"((lane >> 3) * 4096 + (lane & 7) * 8), "v"(f),
                     "s"(ws + OFF_FR + (T.l0 >> 9) * 32768 + ((T.l0 & 511) + 8 * T.nt) * 8) : "memory");
        STAMP(8);
      };
      fresh();
      {
        const int red = layer & ((wv + 1) >> 3) & (bid >> 2);
        char* xin = ws + OFF_X0 + 2 * layer * XBYTES + (size_t)chan_of_block(bid) * (L_ * 2);
        conv_h(smem, xin, ws + OFF_FS + layer * ((int)(OFF_FR - OFF_FS) + FCOPY * 4096), 4096 + layer * 28672,
               ws + OFF_FS + layer * ((int)(OFF_P0 - OFF_FS) + FCOPY * 1024), layer, red, xin + XBYTES, ws + OFF_KINV + (chan_of_block(bid) + (wv & 1)) * 4, budget,
               wv - 4, lane, [&]() AI { if (red) reduce(); } ST_PASS);
      }
      STAMP(12); STAMP_NEXT();
      u32 redo;
#pragma nounroll
      do {
        fresh();
        redo = ffn_h1(smem, ws, ws + OFF_X0 + (2 * layer + 1) * XBYTES, wr, budget, tile_of(bid), wv - 4, lane ST_PASS);
      } while (redo);
      fresh();
      ffn_h2(smem, ws, wr, ws + OFF_X0 + 3 * XBYTES + (size_t)(8 * ((bid & 1) + 2 * (bid >> 3)) + 2 * (wv - 4)) * (L_ * 2) + (bid & 6) * 512 + 2048,
             layer, tile_of(bid), wv - 4, lane ST_PASS);
      if (layer == 0) {
        fresh();
        const Tile T = tile_of(bid);
        const int t = 64 * (wv - 4) + lane;
        u32x4 v[2];
#pragma unroll
        for (int it = 0; it < 2; ++it) v[it] = *(const u32x4*)(smem + FF_OUT + (32 * it + (t >> 3)) * 144 + (t & 7) * 16);
#pragma unroll
        for (int it = 0; it < 2; ++it)
          asm volatile("global_store_dwordx4 %0, %1, %2 sc1\n\ts_nop 1" ::"v"((t >> 3) * (L_ * 2) + (t & 7) * 16), "v"(v[it]),
                       "s"(ws + OFF_X0 + 2 * XBYTES + ((size_t)(64 * T.nt + 32 * it) * L_ + T.l0) * 2) : "memory");
        STAMP(13); STAMP_NEXT();
        nap(((wv == 7 ? NAP_C : NAP_B) >> (4 * (bid & 6))) & 255);
      }
      ++layer;
      ties(layer);
    } while (layer < 2);
  }
  {
    fresh();
    const Tile T = tile_of(bid);
    const int lb = wv & 3, kk = wv >> 2;
    st16_wt(out + (size_t)(T.l0 + 16 * lb + (lane & 15)) * D_ + 64 * T.nt + 32 * kk + 8 * (lane >> 4), out_frag(smem, lane, lb, kk));
  }
  STAMP(13); STAMP_END(1, CL_END);
  asm volatile(".Lmain_end:");
}

extern "C" void kernel_launch(void* const* d_in, const int* in_sizes, int n_in, void* d_out,
                              int out_size, void* d_ws, size_t ws_size, hipStream_t stream) {
  const u16* x0 = (const u16*)d_in[0];
  const u16* logA = (const u16*)d_in[1];
  const u16* Aim = (const u16*)d_in[2];
  const u16* Cre = (const u16*)d_in[3];
  const u16* Cim = (const u16*)d_in[4];
  const u16* logdt = (const u16*)d_in[5];
  const u16* Dsk = (const u16*)d_in[6];
  const u16* w1 = (const u16*)d_in[8];
  const u16* w3 = (const u16*)d_in[9];
  const u16* w2 = (const u16*)d_in[10];
  char* ws = (char*)d_ws;
  prep_kernel<<<PB_END, 256, 0, stream>>>(x0, logA, Aim, Cre, Cim, logdt, Dsk, w1, w3, w2, ws);
  main_kernel<<<256, 512, 0, stream>>>(ws, (u16*)d_out);
}
```
